```python
import torch

def _softmax(x, axis=-1):
    return torch.softmax(x, dim=axis)
def _gelu(x):
    return torch.nn.functional.gelu(x, approximate="tanh")
def _silu(x):
    return torch.nn.functional.silu(x)
def _sigmoid(x):
    return torch.sigmoid(x)
def _layernorm(x, axis=-1, eps=1e-5):
    m = x.mean(dim=axis, keepdim=True)
    v = x.var(dim=axis, keepdim=True, unbiased=False)
    return (x - m) / torch.sqrt(v + eps)
def _scatter_add(tgt, idx, src, axis=0):
    return tgt.clone().index_add_(axis, idx, src)
def _segment_sum(src, seg, n):
    out = torch.zeros((n,) + src.shape[1:], dtype=src.dtype)
    return out.index_add_(0, seg, src)
def _pad(x, pw):
    flat = tuple(v for pair in reversed(pw) for v in pair)
    return torch.nn.functional.pad(x, flat)

kernel_name = 'concat_heads__softmax_full_mha__sliding_block__prompt'

def setup_inputs():

    _w = (lambda a: (lambda t: t.to(torch.bfloat16) if t.is_floating_point() else t)(torch.as_tensor(a)))
    def _A_setup():
        B, S, D = 1, 2048, 512
        return {
            "x": _w((lambda *s: torch.randn(*s, dtype=torch.bfloat16) * 0.2)(B, S, D)),
            "wqkv": _w((lambda *s: torch.randn(*s, dtype=torch.bfloat16) * 0.4)(D, 3 * D)),
            "wo": _w((lambda *s: torch.randn(*s, dtype=torch.bfloat16) * 0.1)(D, D)),
        }
    _A_in = _A_setup()

    def _B_setup():
        B, S, D = 1, 2048, 512
        return {
            "x": _w((lambda *s: torch.randn(*s, dtype=torch.bfloat16) * 0.4)(B, S, D)),
            "wqkv": _w((lambda *s: torch.randn(*s, dtype=torch.bfloat16) * 0.4)(D, 3 * D)),
            "wo": _w((lambda *s: torch.randn(*s, dtype=torch.bfloat16) * 0.05)(D, D)),
        }
    _B_in = _B_setup()

    return {
        "A_x": _A_in["x"],
        "A_wqkv": _A_in["wqkv"],
        "A_wo": _A_in["wo"],
        "B_x": _B_in["x"],
        "B_wqkv": _B_in["wqkv"],
        "B_wo": _B_in["wo"],
    }

def reference(A_x, A_wqkv, A_wo, B_x, B_wqkv, B_wo):
    def _A_ref(x, wqkv, wo):
        B, S, D = x.shape
        H = 32; Dh = D // H
        qkv = (lambda x, *d: x.permute(*d))((x @ wqkv).reshape(B, S, 3, H, Dh), 2, 0, 3, 1, 4)
        q, k, v = qkv[0], qkv[1], qkv[2]
        scale = Dh ** -0.5
        scores = torch.einsum("bhqd,bhkd->bhqk", q, k) * scale
        p = _softmax(scores, axis=-1)
        o = torch.einsum("bhqk,bhkd->bhqd", p, v)
        return (lambda x, *d: x.permute(*d))(o, 0, 2, 1, 3).reshape(B, S, D) @ wo

    def _B_ref(x, wqkv, wo):
        B, S, D = x.shape
        H = 32; Dh = D // H; W = 256
        qkv = (lambda x, *d: x.permute(*d))((x @ wqkv).reshape(B, S, 3, H, Dh), 2, 0, 3, 1, 4)
        q, k, v = qkv[0], qkv[1], qkv[2]
        scores = torch.einsum("bhqd,bhkd->bhqk", q, k) * (Dh ** -0.5)
        idx = torch.arange(S)
        mask = (idx[:, None] - idx[None, :] >= 0) & (idx[:, None] - idx[None, :] < W)
        scores = torch.where(mask[None, None], scores, -1e9)
        p = _softmax(scores, axis=-1)
        o = torch.einsum("bhqk,bhkd->bhqd", p, v)
        return (lambda x, *d: x.permute(*d))(o, 0, 2, 1, 3).reshape(B, S, D) @ wo

    a = _A_ref(A_x, A_wqkv, A_wo)
    b = _B_ref(B_x, B_wqkv, B_wo)
    return torch.concatenate([a, b], axis=-1)
```

```cpp
#include <hip/hip_runtime.h>
#include <hip/hip_bf16.h>
#include <stdint.h>

typedef __bf16 bf16x8 __attribute__((ext_vector_type(8)));
typedef __bf16 bf16x2 __attribute__((ext_vector_type(2)));
typedef short s16x4 __attribute__((ext_vector_type(4)));
typedef short s16x8 __attribute__((ext_vector_type(8)));
typedef float f32x16 __attribute__((ext_vector_type(16)));
typedef float f32x2 __attribute__((ext_vector_type(2)));
typedef unsigned u32x4 __attribute__((ext_vector_type(4)));
typedef unsigned u32x2 __attribute__((ext_vector_type(2)));
#define LDSP __attribute__((address_space(3)))
#define DI __device__ __forceinline__

constexpr int SEQ = 2048, DM = 512, NH = 32;
constexpr size_t TB = 2u << 20;
constexpr size_t WS_Q = 0, WS_K = 2 * TB, WS_V = 4 * TB, WS_O = 6 * TB;
constexpr float QSCALE = 0.25f * 1.4426950408889634f;

DI unsigned cvtpk(float lo, float hi) {
    f32x2 v = {lo, hi};
    bf16x2 b = __builtin_convertvector(v, bf16x2);
    return __builtin_bit_cast(unsigned, b);
}
DI f32x16 mfma(bf16x8 a, bf16x8 b, f32x16 c) { return __builtin_amdgcn_mfma_f32_32x32x16_bf16(a, b, c, 0, 0, 0); }
DI void glds16(const void* sbase, unsigned voff, unsigned lds_base) {
    unsigned sv;
    asm volatile("s_mov_b32 %0, m0\n\ts_mov_b32 m0, %3\n\ts_nop 0\n\tglobal_load_lds_dwordx4 %1, %2\n\ts_mov_b32 m0, %0"
                 : "=&s"(sv) : "v"(voff), "s"(sbase), "s"(lds_base) : "memory");
}
DI int rfl(int x) { return __builtin_amdgcn_readfirstlane(x); }
DI bf16x8 lds_frag(const LDSP char* p) { return *(const LDSP bf16x8*)p; }
DI s16x4 lds_tr(const LDSP char* p) { return __builtin_amdgcn_ds_read_tr16_b64_v4i16((LDSP s16x4*)p); }
DI bf16x8 cat44(s16x4 a, s16x4 b) {
    s16x8 r = {a[0], a[1], a[2], a[3], b[0], b[1], b[2], b[3]};
    return __builtin_bit_cast(bf16x8, r);
}
#define WAIT_BAR(N) asm volatile("s_waitcnt vmcnt(" #N ") lgkmcnt(0)\n\ts_barrier" ::: "memory")

constexpr int G1_STAGE = 40960, G1_XB = 16384;

template <int VMASK>
DI void qkv_body(const char* X, const char* W, char* ws, int br, int tb, int cb, LDSP char* lds) {
    const int tid = threadIdx.x, lane = tid & 63, w = rfl(tid >> 6);
    const int wm = w & 1, wn = w >> 1;
    const unsigned lds0 = (unsigned)(uintptr_t)lds;
    unsigned xo[4], wo[6];
#pragma unroll
    for (int i = 0; i < 4; ++i) {
        const int row = 8 * (4 * w + i) + (lane >> 3), sl = (lane & 7) ^ ((row >> 1) & 7);
        xo[i] = (unsigned)(tb * 128 + row) * 1024u + sl * 16;
    }
#pragma unroll
    for (int i = 0; i < 6; ++i) {
        const int o = (6 * w + i) * 1024 + lane * 16, k = o / 384, rem = o % 384, g = (rem >> 6) ^ ((k >> 1) & 1);
        wo[i] = (unsigned)k * 3072u + (unsigned)(cb * 192 + g * 32) * 2u + (rem & 63);
    }
    auto issue = [&](int s) {
        const unsigned slot = lds0 + (s & 3) * G1_STAGE;
#pragma unroll
        for (int i = 0; i < 4; ++i) glds16(X + s * 128, xo[i], slot + (4 * w + i) * 1024);
#pragma unroll
        for (int i = 0; i < 6; ++i) glds16(W + (size_t)s * 64 * 3072, wo[i], slot + G1_XB + (6 * w + i) * 1024);
    };
    issue(0); issue(1); issue(2);
    const int t32 = lane & 31, h = lane >> 5;
    unsigned xa[4], wa[3];
#pragma unroll
    for (int ks = 0; ks < 4; ++ks) xa[ks] = (unsigned)(wm * 64 + t32) * 128u + (((2 * ks + h) ^ ((t32 >> 1) & 7)) * 16);
    {
        const int g16 = lane >> 4, q = (lane & 15) >> 2, p = lane & 3;
#pragma unroll
        for (int c = 0; c < 3; ++c)
            wa[c] = G1_XB + (unsigned)(8 * h + q) * 384u + (((3 * wn + c) ^ ((q >> 1) & 1)) * 64) + (16 * (g16 & 1) + 4 * p) * 2;
    }
    f32x16 acc[2][3];
#pragma unroll
    for (int a = 0; a < 2; ++a)
#pragma unroll
        for (int c = 0; c < 3; ++c) acc[a][c] = f32x16{};
#pragma unroll
    for (int s = 0; s < 8; ++s) {
        if (s <= 5) WAIT_BAR(20); else if (s == 6) WAIT_BAR(10); else WAIT_BAR(0);
        if (s + 3 < 8) issue(s + 3);
        const LDSP char* sb = lds + (s & 3) * G1_STAGE;
#pragma unroll
        for (int ks = 0; ks < 4; ++ks) {
            bf16x8 xf[2], wf[3];
#pragma unroll
            for (int a = 0; a < 2; ++a) xf[a] = lds_frag(sb + xa[ks] + a * 4096);
#pragma unroll
            for (int c = 0; c < 3; ++c) wf[c] = cat44(lds_tr(sb + wa[c] + ks * 16 * 384), lds_tr(sb + wa[c] + (ks * 16 + 4) * 384));
#pragma unroll
            for (int a = 0; a < 2; ++a)
#pragma unroll
                for (int c = 0; c < 3; ++c)
                    acc[a][c] = ((VMASK >> c) & 1) ? mfma(xf[a], wf[c], acc[a][c]) : mfma(wf[c], xf[a], acc[a][c]);
        }
    }
#pragma unroll
    for (int c = 0; c < 3; ++c) {
        const int n0 = cb * 192 + wn * 96 + c * 32, type = n0 >> 9, hd0 = (n0 & 511) >> 4;
#pragma unroll
        for (int a = 0; a < 2; ++a) {
            const int tile = tb * 4 + wm * 2 + a;
            if ((VMASK >> c) & 1) {
                char* dst = ws + WS_V + (size_t)br * TB + (size_t)((hd0 + (t32 >> 4)) * 64 + tile) * 1024 + h * 256 + (t32 & 15) * 16;
#pragma unroll
                for (int s = 0; s < 2; ++s) {
                    u32x4 v;
#pragma unroll
                    for (int i = 0; i < 4; ++i) v[i] = cvtpk(acc[a][c][8 * s + 2 * i], acc[a][c][8 * s + 2 * i + 1]);
                    *(u32x4*)(dst + s * 512) = v;
                }
            } else {
                const float sc = type == 0 ? QSCALE : 1.0f;
                char* dst = ws + (type == 0 ? WS_Q : WS_K) + (size_t)br * TB + (size_t)(hd0 * 64 + tile) * 1024 + lane * 16;
#pragma unroll
                for (int u = 0; u < 2; ++u) {
                    u32x4 v;
#pragma unroll
                    for (int i = 0; i < 4; ++i) v[i] = cvtpk(acc[a][c][8 * u + 2 * i] * sc, acc[a][c][8 * u + 2 * i + 1] * sc);
                    *(u32x4*)(dst + (size_t)u * 65536) = v;
                }
            }
        }
    }
}

__global__ __launch_bounds__(256) void k_qkv(const char* xA, const char* wA, const char* xB, const char* wB, char* ws) {
    extern __shared__ __attribute__((aligned(16))) char smem[];
    LDSP char* lds = (LDSP char*)smem;
    const int bid = blockIdx.x, xcd = bid & 7, idx = bid >> 3, br = xcd >> 2, xs = xcd & 3;
    const int tb = (xs & 1) * 8 + (idx & 7), cb = (xs >> 1) * 4 + (idx >> 3);
    const char* X = br ? xB : xA;
    const char* W = br ? wB : wA;
    const int g = 2 * cb + (rfl(threadIdx.x >> 6) >> 1);
    if (g < 10) qkv_body<0>(X, W, ws, br, tb, cb, lds);
    else if (g == 10) qkv_body<4>(X, W, ws, br, tb, cb, lds);
    else qkv_body<7>(X, W, ws, br, tb, cb, lds);
}

constexpr unsigned L_AK = 0, L_AV = 65536, L_BK = 131072, L_BV = 147456;
constexpr float SAFE_L = 1.2676506e30f;
constexpr float SAFE_O = 1.3292280e36f;
constexpr float NEG_BIG = -3.0e38f;

DI float xhalf_max(float m) {
    auto r = __builtin_amdgcn_permlane32_swap(__float_as_uint(m), __float_as_uint(m), false, false);
    return fmaxf(__uint_as_float(r[0]), __uint_as_float(r[1]));
}
DI float max16(const f32x16& s) {
    float a = fmaxf(fmaxf(s[0], s[1]), s[2]), b = fmaxf(fmaxf(s[3], s[4]), s[5]);
    a = fmaxf(fmaxf(a, s[6]), s[7]); b = fmaxf(fmaxf(b, s[8]), s[9]);
    a = fmaxf(fmaxf(a, s[10]), s[11]); b = fmaxf(fmaxf(b, s[12]), s[13]);
    a = fmaxf(fmaxf(a, s[14]), s[15]);
    return fmaxf(a, b);
}
DI f32x16 bcast16(float v) { f32x16 r; for (int i = 0; i < 16; ++i) r[i] = v; return r; }
DI void load_v(u32x4& v0, u32x4& v1, unsigned addr) {
    asm volatile("s_mov_b64 exec, %3\n\tds_read_b128 %0, %2\n\tds_read_b128 %1, %2 offset:512\n\ts_mov_b64 exec, -1\n\ts_waitcnt lgkmcnt(0)"
                 : "+v"(v0), "+v"(v1) : "v"(addr), "s"(0x0000FFFF0000FFFFull) : "memory");
}
DI void pv_tile(const f32x16& p, f32x16& o, u32x4& v0, u32x4& v1, unsigned vaddr) {
    u32x4 p0, p1;
#pragma unroll
    for (int i = 0; i < 4; ++i) { p0[i] = cvtpk(p[2 * i], p[2 * i + 1]); p1[i] = cvtpk(p[8 + 2 * i], p[9 + 2 * i]); }
    load_v(v0, v1, vaddr);
    o = mfma(__builtin_bit_cast(bf16x8, v0), __builtin_bit_cast(bf16x8, p0), o);
    o = mfma(__builtin_bit_cast(bf16x8, v1), __builtin_bit_cast(bf16x8, p1), o);
}
DI void mask_tile(f32x16& s, int mode, int z) {
    if (mode == 0) return;
#pragma unroll
    for (int r = 0; r < 16; ++r) {
        const int cr = (r & 3) + 8 * (r >> 2);
        const bool keep = mode == 1 ? (cr <= z) : (cr > z);
        s[r] = keep ? s[r] : -INFINITY;
    }
}
DI bool unsafe_state(const f32x16& o) {
    float mx = fabsf(o[0]);
#pragma unroll
    for (int i = 1; i < 8; ++i) mx = fmaxf(mx, fabsf(o[i]));
    bool nan = false;
#pragma unroll
    for (int i = 0; i < 9; ++i) nan |= !(o[i] == o[i]);
    return nan || !(mx < SAFE_O) || !(o[8] < SAFE_L);
}
DI void safe_tiles(const LDSP char* lds, unsigned kbase, unsigned vbase, int t0, int n, int step, int first_mode, int last_mode,
                   bf16x8 q, int lane, int z, f32x16& o, float& m) {
    const unsigned vl = (lane >> 5) * 256 + (lane & 15) * 16;
    u32x4 v0 = {0x3F803F80u, 0x3F803F80u, 0x3F803F80u, 0x3F803F80u}, v1 = v0;
    o = f32x16{}; m = NEG_BIG;
    for (int i = 0; i < n; ++i) {
        const int t = t0 + i * step;
        f32x16 s = mfma(lds_frag(lds + kbase + t * 1024 + lane * 16), q, f32x16{});
        mask_tile(s, i == 0 ? first_mode : (i == n - 1 ? last_mode : 0), z);
        const float mn = fmaxf(m, xhalf_max(max16(s)));
        const float al = __builtin_amdgcn_exp2f(m - mn);
        m = mn;
#pragma unroll
        for (int r = 0; r < 16; ++r) { o[r] *= al; s[r] = __builtin_amdgcn_exp2f(s[r] - mn); }
        pv_tile(s, o, v0, v1, (unsigned)(uintptr_t)lds + vbase + t * 1024 + vl);
    }
}

#define A_TILE(C, N, KOFF, VOFF) \
    "s_waitcnt lgkmcnt(2)\n\t" \
    "v_mfma_f32_32x32x16_bf16 v[" #N ":" #N "+15], v[68:71], v[64:67], v[80:95]\n\t" \
    "ds_read_b128 v[68:71], v30 offset:" #KOFF "\n\t" \
    "v_exp_f32 v[" #C "+0], v[" #C "+0]\n\t" \
    "v_exp_f32 v[" #C "+1], v[" #C "+1]\n\t" \
    "v_exp_f32 v[" #C "+2], v[" #C "+2]\n\t" \
    "v_cvt_pk_bf16_f32 v[" #C "+0], v[" #C "+0], v[" #C "+1]\n\t" \
    "v_exp_f32 v[" #C "+3], v[" #C "+3]\n\t" \
    "v_exp_f32 v[" #C "+4], v[" #C "+4]\n\t" \
    "v_cvt_pk_bf16_f32 v[" #C "+1], v[" #C "+2], v[" #C "+3]\n\t" \
    "v_exp_f32 v[" #C "+5], v[" #C "+5]\n\t" \
    "v_exp_f32 v[" #C "+6], v[" #C "+6]\n\t" \
    "v_cvt_pk_bf16_f32 v[" #C "+2], v[" #C "+4], v[" #C "+5]\n\t" \
    "v_exp_f32 v[" #C "+7], v[" #C "+7]\n\t" \
    "v_exp_f32 v[" #C "+8], v[" #C "+8]\n\t" \
    "v_cvt_pk_bf16_f32 v[" #C "+3], v[" #C "+6], v[" #C "+7]\n\t" \
    "v_exp_f32 v[" #C "+9], v[" #C "+9]\n\t" \
    "v_exp_f32 v[" #C "+10], v[" #C "+10]\n\t" \
    "s_waitcnt lgkmcnt(2)\n\t" \
    "v_mfma_f32_32x32x16_bf16 v[96:111], v[72:75], v[" #C ":" #C "+3], v[96:111]\n\t" \
    "v_cvt_pk_bf16_f32 v[" #C "+4], v[" #C "+8], v[" #C "+9]\n\t" \
    "v_exp_f32 v[" #C "+11], v[" #C "+11]\n\t" \
    "v_exp_f32 v[" #C "+12], v[" #C "+12]\n\t" \
    "v_cvt_pk_bf16_f32 v[" #C "+5], v[" #C "+10], v[" #C "+11]\n\t" \
    "v_exp_f32 v[" #C "+13], v[" #C "+13]\n\t" \
    "v_exp_f32 v[" #C "+14], v[" #C "+14]\n\t" \
    "v_exp_f32 v[" #C "+15], v[" #C "+15]\n\t" \
    "v_cvt_pk_bf16_f32 v[" #C "+6], v[" #C "+12], v[" #C "+13]\n\t" \
    "v_cvt_pk_bf16_f32 v[" #C "+7], v[" #C "+14], v[" #C "+15]\n\t" \
    "s_waitcnt lgkmcnt(1)\n\t" \
    "s_nop 0\n\t" \
    "v_mfma_f32_32x32x16_bf16 v[96:111], v[76:79], v[" #C "+4:" #C "+7], v[96:111]\n\t" \
    "s_mov_b64 exec, %[msk]\n\t" \
    "ds_read_b128 v[72:75], v31 offset:" #VOFF "\n\t" \
    "ds_read_b128 v[76:79], v31 offset:" #VOFF "+512\n\t" \
    "s_mov_b64 exec, -1\n\t"

DI void run_a(unsigned kaddr, unsigned vaddr, int n4, bf16x8 q, const f32x16& nm, f32x16& o) {
    u32x4 v0 = {0x3F803F80u, 0x3F803F80u, 0x3F803F80u, 0x3F803F80u}, v1 = v0;
    f32x16 s0, s1;
    u32x4 kf;
    asm volatile(
        "ds_read_b128 v[68:71], v30\n\t"
        "s_waitcnt lgkmcnt(0)\n\t"
        "v_mfma_f32_32x32x16_bf16 v[32:47], v[68:71], v[64:67], v[80:95]\n\t"
        "ds_read_b128 v[68:71], v30 offset:1024\n\t"
        "s_mov_b64 exec, %[msk]\n\t"
        "ds_read_b128 v[72:75], v31\n\t"
        "ds_read_b128 v[76:79], v31 offset:512\n\t"
        "s_mov_b64 exec, -1\n\t"
        "s_nop 7\n\t"
        "s_nop 3\n\t"
        "1:\n\t"
        A_TILE(32, 48, 2048, 1024)
        A_TILE(48, 32, 3072, 2048)
        A_TILE(32, 48, 4096, 3072)
        A_TILE(48, 32, 5120, 4096)
        "v_add_u32 v30, 0x1000, v30\n\t"
        "v_add_u32 v31, 0x1000, v31\n\t"
        "s_sub_u32 %[n], %[n], 1\n\t"
        "s_cmp_lg_u32 %[n], 0\n\t"
        "s_cbranch_scc1 1b\n\t"
        "s_waitcnt lgkmcnt(0)\n\t"
        "s_nop 15\n\t"
        : "+{v30}"(kaddr), "+{v31}"(vaddr), "=&{v[32:47]}"(s0), "=&{v[48:63]}"(s1), "=&{v[68:71]}"(kf), "+{v[72:75]}"(v0), "+{v[76:79]}"(v1),
          "+{v[96:111]}"(o), [n] "+s"(n4)
        : "{v[64:67]}"(q), "{v[80:95]}"(nm), [msk] "s"(0x0000FFFF0000FFFFull)
        : "memory", "scc");
}

DI void store_o(char* ws, int br, int qtile, int head, int lane, const f32x16& o) {
    const float inv = 1.0f / o[8];
    u32x4 v;
#pragma unroll
    for (int i = 0; i < 4; ++i) v[i] = cvtpk(o[2 * i] * inv, o[2 * i + 1] * inv);
    *(u32x4*)(ws + WS_O + (size_t)br * TB + (size_t)(qtile * 32 + head) * 1024 + lane * 16) = v;
}

__global__ __launch_bounds__(1024) void k_attn(char* ws) {
    extern __shared__ __attribute__((aligned(16))) char smem[];
    LDSP char* lds = (LDSP char*)smem;
    const unsigned lds0 = (unsigned)(uintptr_t)lds;
    const int tid = threadIdx.x, lane = tid & 63, w = rfl(tid >> 6);
    const int bid = blockIdx.x, head = (bid & 7) * 4 + (bid >> 6), jb = (bid >> 3) & 7;
    const int qt = w & 7, cls = w >> 3;
    const int Qt = jb * 8 + qt;
    const char* KA = ws + WS_K + (size_t)head * 65536;
    const char* VA = ws + WS_V + (size_t)head * 65536;
    const char* KB = KA + TB;
    const char* VB = VA + TB;
    const unsigned lo = lane * 16;
    bf16x8 qa, qb;
    {
        const char* qp = ws + WS_Q + (size_t)(head * 64 + Qt) * 1024;
        asm volatile("global_load_dwordx4 %0, %2, %3\n\tglobal_load_dwordx4 %1, %2, %4" : "=&v"(qa), "=&v"(qb) : "v"(lo), "s"(qp), "s"(qp + TB) : "memory");
    }
    {
        int tbw = jb * 8 - 8 + w; if (tbw < 0) tbw = 0;
        glds16(KB + (size_t)tbw * 1024, lo, lds0 + L_BK + w * 1024);
        glds16(VB + (size_t)tbw * 1024, lo, lds0 + L_BV + w * 1024);
        glds16(KA + (size_t)(28 + w) * 1024, lo, lds0 + L_AK + (28 + w) * 1024);
        glds16(VA + (size_t)(28 + w) * 1024, lo, lds0 + L_AV + (28 + w) * 1024);
#pragma unroll
        for (int i = 0; i < 3; ++i) {
            int t = i * 16 + w; if (t >= 28) t += 16;
            glds16(KA + (size_t)t * 1024, lo, lds0 + L_AK + t * 1024);
            glds16(VA + (size_t)t * 1024, lo, lds0 + L_AV + t * 1024);
        }
    }
    asm volatile("s_waitcnt vmcnt(6)\n\ts_barrier" : "+v"(qa), "+v"(qb) :: "memory");
    const int z = (lane & 31) - 4 * (lane >> 5);
    const unsigned vl = (lane >> 5) * 256 + (lane & 15) * 16;
    f32x16 oa = f32x16{};
    float ma;
    f32x16 nma;
    if (cls == 0) {
        const int nt = Qt < 8 ? Qt + 1 : 9;
        f32x16 o = f32x16{};
        u32x4 v0 = {0x3F803F80u, 0x3F803F80u, 0x3F803F80u, 0x3F803F80u}, v1 = v0;
        int t = qt + 8;
        f32x16 s = mfma(lds_frag(lds + L_BK + t * 1024 + lo), qb, f32x16{});
        mask_tile(s, 1, z);
        float m = xhalf_max(max16(s));
#pragma unroll
        for (int r = 0; r < 16; ++r) s[r] = __builtin_amdgcn_exp2f(s[r] - m);
        pv_tile(s, o, v0, v1, lds0 + L_BV + t * 1024 + vl);
        const f32x16 nm = bcast16(-m);
        const int nmid = nt - 1 < 7 ? nt - 1 : 7;
        for (int i = 0; i < nmid; ++i) {
            --t;
            s = mfma(lds_frag(lds + L_BK + t * 1024 + lo), qb, nm);
#pragma unroll
            for (int r = 0; r < 16; ++r) s[r] = __builtin_amdgcn_exp2f(s[r]);
            pv_tile(s, o, v0, v1, lds0 + L_BV + t * 1024 + vl);
        }
        if (nt == 9) {
            --t;
            s = mfma(lds_frag(lds + L_BK + t * 1024 + lo), qb, nm);
            mask_tile(s, 2, z);
#pragma unroll
            for (int r = 0; r < 16; ++r) s[r] = __builtin_amdgcn_exp2f(s[r]);
            pv_tile(s, o, v0, v1, lds0 + L_BV + t * 1024 + vl);
        }
        if (__any(unsafe_state(o))) safe_tiles(lds, L_BK, L_BV, qt + 8, nt, -1, 1, nt == 9 ? 2 : 0, qb, lane, z, o, m);
        store_o(ws, 1, Qt, head, lane, o);
        asm volatile("s_waitcnt vmcnt(1)\n\ts_barrier" ::: "memory");
        f32x16 s0 = mfma(lds_frag(lds + L_AK + lo), qa, f32x16{});
        ma = xhalf_max(max16(s0));
        nma = bcast16(-ma);
        run_a(lds0 + L_AK + lo, lds0 + L_AV + vl, 7, qa, nma, oa);
    } else {
        f32x16 s0 = mfma(lds_frag(lds + L_AK + 28 * 1024 + lo), qa, f32x16{});
        ma = xhalf_max(max16(s0));
        nma = bcast16(-ma);
        run_a(lds0 + L_AK + 28 * 1024 + lo, lds0 + L_AV + 28 * 1024 + vl, 2, qa, nma, oa);
        asm volatile("s_waitcnt vmcnt(0)\n\ts_barrier" ::: "memory");
        run_a(lds0 + L_AK + 36 * 1024 + lo, lds0 + L_AV + 36 * 1024 + vl, 7, qa, nma, oa);
    }
    if (__any(unsafe_state(oa))) safe_tiles(lds, L_AK, L_AV, cls ? 28 : 0, cls ? 36 : 28, 1, 0, 0, qa, lane, z, oa, ma);
    LDSP float* ex = (LDSP float*)(lds + L_BK) + qt * 640 + lane;
    if (cls == 1) {
#pragma unroll
        for (int r = 0; r < 9; ++r) ex[r * 64] = oa[r];
        ex[9 * 64] = ma;
    }
    asm volatile("s_waitcnt lgkmcnt(0)\n\ts_barrier" ::: "memory");
    if (cls == 0) {
        const float m1 = ex[9 * 64], mn = fmaxf(ma, m1);
        const float s0 = __builtin_amdgcn_exp2f(ma - mn), s1 = __builtin_amdgcn_exp2f(m1 - mn);
#pragma unroll
        for (int r = 0; r < 9; ++r) oa[r] = oa[r] * s0 + ex[r * 64] * s1;
        store_o(ws, 0, Qt, head, lane, oa);
    }
}

__global__ __launch_bounds__(256) void k_out(const char* woA, const char* woB, const char* ws, char* out) {
    extern __shared__ __attribute__((aligned(16))) char smem[];
    LDSP char* lds = (LDSP char*)smem;
    const unsigned lds0 = (unsigned)(uintptr_t)lds;
    const int tid = threadIdx.x, lane = tid & 63, w = rfl(tid >> 6);
    const int bid = blockIdx.x, xcd = bid & 7, idx = bid >> 3, br = xcd >> 2;
    const int qb = (xcd & 3) * 4 + (idx & 3), nb = idx >> 2;
    const char* Wo = br ? woB : woA;
    {
        const int r = lane >> 3, sl = (lane & 7) ^ (((r >> 1) & 1) << 2);
        const unsigned vo = (unsigned)r * 1024u + nb * 128 + sl * 16;
#pragma unroll
        for (int i = 0; i < 16; ++i) glds16(Wo + (size_t)(w * 16 + i) * 8192, vo, lds0 + (w * 16 + i) * 1024);
    }
    const char* Of = ws + WS_O + (size_t)br * TB + (size_t)(qb * 4 + w) * 32768 + lane * 16;
    bf16x8 of[32];
#pragma unroll
    for (int hd = 0; hd < 32; ++hd) of[hd] = *(const bf16x8*)(Of + hd * 1024);
    const int h = lane >> 5, g1 = (lane >> 4) & 1, q = (lane & 15) >> 2, p = lane & 3;
    unsigned wa[2];
#pragma unroll
    for (int nt = 0; nt < 2; ++nt) {
        const int colb = (nt * 32 + 16 * (p & 1) + 8 * g1 + 4 * (p >> 1)) * 2;
        wa[nt] = (unsigned)(4 * h + q) * 128u + (colb ^ (((q >> 1) & 1) << 6));
    }
    WAIT_BAR(0);
    f32x16 acc[2] = {f32x16{}, f32x16{}};
#pragma unroll
    for (int hd = 0; hd < 32; ++hd)
#pragma unroll
        for (int nt = 0; nt < 2; ++nt) {
            const bf16x8 wf = cat44(lds_tr(lds + wa[nt] + hd * 2048), lds_tr(lds + wa[nt] + hd * 2048 + 1024));
            acc[nt] = mfma(wf, of[hd], acc[nt]);
        }
    char* dst = out + (size_t)((qb * 4 + w) * 32 + (lane & 31)) * 2048 + br * 1024 + nb * 128 + h * 32;
#pragma unroll
    for (int nt = 0; nt < 2; ++nt)
#pragma unroll
        for (int u = 0; u < 2; ++u) {
            u32x4 v;
#pragma unroll
            for (int i = 0; i < 4; ++i) v[i] = cvtpk(acc[nt][8 * u + 2 * i], acc[nt][8 * u + 2 * i + 1]);
            *(u32x4*)(dst + nt * 64 + u * 16) = v;
        }
}

extern "C" void kernel_launch(void* const* d_in, const int* in_sizes, int n_in, void* d_out, int out_size, void* d_ws, size_t ws_size,
                              hipStream_t stream) {
    const char* xA = (const char*)d_in[0];
    const char* wA = (const char*)d_in[1];
    const char* oA = (const char*)d_in[2];
    const char* xB = (const char*)d_in[3];
    const char* wB = (const char*)d_in[4];
    const char* oB = (const char*)d_in[5];
    char* ws = (char*)d_ws;
    (void)hipFuncSetAttribute((const void*)k_qkv, hipFuncAttributeMaxDynamicSharedMemorySize, 4 * G1_STAGE);
    (void)hipFuncSetAttribute((const void*)k_attn, hipFuncAttributeMaxDynamicSharedMemorySize, 163840);
    (void)hipFuncSetAttribute((const void*)k_out, hipFuncAttributeMaxDynamicSharedMemorySize, 65536);
    k_qkv<<<256, 256, 4 * G1_STAGE, stream>>>(xA, wA, xB, wB, ws);
    k_attn<<<256, 1024, 163840, stream>>>(ws);
    k_out<<<256, 256, 65536, stream>>>(oA, oB, ws, (char*)d_out);
}
```

```cpp
#include <hip/hip_runtime.h>
#include <hip/hip_bf16.h>
#include <stdint.h>
#include <type_traits>

typedef __bf16 bf16x8 __attribute__((ext_vector_type(8)));
typedef __bf16 bf16x2 __attribute__((ext_vector_type(2)));
typedef short s16x4 __attribute__((ext_vector_type(4)));
typedef short s16x8 __attribute__((ext_vector_type(8)));
typedef float f32x16 __attribute__((ext_vector_type(16)));
typedef float f32x4 __attribute__((ext_vector_type(4)));
typedef float f32x2 __attribute__((ext_vector_type(2)));
typedef unsigned u32x4 __attribute__((ext_vector_type(4)));
#define LDSP __attribute__((address_space(3)))
#define DI __device__ __forceinline__

constexpr size_t TB = 2u << 20;
constexpr size_t WS_Q = 0, WS_K = 2 * TB, WS_V = 4 * TB, WS_O = 6 * TB;
constexpr float QSCALE = 0.25f * 1.4426950408889634f;

DI unsigned cvtpk(float lo, float hi) {
    f32x2 v = {lo, hi};
    bf16x2 b = __builtin_convertvector(v, bf16x2);
    return __builtin_bit_cast(unsigned, b);
}
DI f32x16 mfma(bf16x8 a, bf16x8 b, f32x16 c) { return __builtin_amdgcn_mfma_f32_32x32x16_bf16(a, b, c, 0, 0, 0); }
DI int rfl(int x) { return __builtin_amdgcn_readfirstlane(x); }
DI const char* uni(const void* p) {
    const uint64_t a = (uint64_t)p;
    return (const char*)(((uint64_t)(unsigned)rfl((int)(a >> 32)) << 32) | (unsigned)rfl((int)a));
}
DI void glds16(const void* sbase, unsigned voff, unsigned lds_base) {
    unsigned sv;
    asm volatile("s_mov_b32 %0, m0\n\ts_mov_b32 m0, %3\n\ts_nop 2\n\tglobal_load_lds_dwordx4 %1, %2\n\ts_mov_b32 m0, %0"
                 : "=&s"(sv) : "v"(voff), "s"(uni(sbase)), "s"(rfl((int)lds_base)) : "memory");
}
DI int fresh_lane() { int l; asm volatile("v_mbcnt_lo_u32_b32 %0, -1, 0\n\tv_mbcnt_hi_u32_b32 %0, -1, %0" : "=v"(l)); return l; }
DI bf16x8 lds_frag(const LDSP char* p) { return *(const LDSP bf16x8*)p; }
DI s16x4 lds_tr(const LDSP char* p) { return __builtin_amdgcn_ds_read_tr16_b64_v4i16((LDSP s16x4*)p); }
DI bf16x8 cat44(s16x4 a, s16x4 b) {
    s16x8 r = {a[0], a[1], a[2], a[3], b[0], b[1], b[2], b[3]};
    return __builtin_bit_cast(bf16x8, r);
}
#define STAMP(i)
#define RSTAMP(i)
#define STAMPX(i)
#define WAIT_BAR(N) asm volatile("s_waitcnt vmcnt(" #N ") lgkmcnt(0)\n\ts_barrier" ::: "memory")
#define WAITV_BAR(N) asm volatile("s_waitcnt vmcnt(" #N ")\n\ts_barrier" ::: "memory")

constexpr size_t WS_F = 8 * TB;
constexpr size_t F_EPOCH = WS_F, F_G = WS_F + 4096, F_O = F_G + 32768;
constexpr int SPIN_MAX = 1 << 18;
typedef __attribute__((address_space(1))) unsigned gu32;
DI unsigned ld_flag(const char* p) { return __hip_atomic_load((gu32*)p, __ATOMIC_RELAXED, __HIP_MEMORY_SCOPE_AGENT); }
DI void st_flag(char* p, unsigned v) { __hip_atomic_store((gu32*)p, v, __ATOMIC_RELAXED, __HIP_MEMORY_SCOPE_AGENT); }
DI void st16_wt(char* p, u32x4 v) { asm volatile("global_store_dwordx4 %0, %1, off sc1\n\ts_nop 1" :: "v"(p), "v"(v) : "memory"); }
DI void acquire_agent() {
    __builtin_amdgcn_fence(__ATOMIC_ACQUIRE, "agent");
    asm volatile("s_waitcnt vmcnt(0)" ::: "memory");
}
DI u32x4 ld16_dev(const char* sbase, unsigned voff) {
    u32x4 r;
    asm volatile("s_nop 4\n\tglobal_load_dwordx4 %0, %1, %2 sc1" : "=&v"(r) : "v"(voff), "s"(uni(sbase)) : "memory");
    return r;
}
DI void poll_flags(const char* p, bool want, unsigned token) {
    for (int it = 0; it < SPIN_MAX; ++it) {
        const unsigned v = want ? ld_flag(p) : token;
        if (__all(v == token)) break;
        __builtin_amdgcn_s_sleep(4);
    }
}
DI const char* gflag_addr(const char* ws, int br, int head, int jb, int lane) {
    const int type = lane < 16 ? 1 : (lane < 32 ? 2 : 0), cb = (type * 512 + head * 16) / 96;
    return ws + F_G + ((br * 8 + jb) * 16 + cb) * 128 + (lane & 15) * 4;
}

constexpr unsigned L_AK = 0, L_AV = 65536, L_BK = 131072, L_BV = 147456;
DI void lds_st16(LDSP char* p, u32x4 v) { *(LDSP u32x4*)p = v; }
template <int N>
DI void stage_window(const char* ws, LDSP char* lds, int head, int jb, int first, int lane) {
    const char* KB = ws + WS_K + TB + (size_t)head * 65536;
    const char* VB = ws + WS_V + TB + (size_t)head * 65536;
    u32x4 kk[N], vv[N];
#pragma unroll
    for (int i = 0; i < N; ++i) {
        int t = jb * 8 - 8 + first + i; if (t < 0) t = 0;
        kk[i] = ld16_dev(KB + (size_t)t * 1024, lane * 16); vv[i] = ld16_dev(VB + (size_t)t * 1024, lane * 16);
    }
#pragma unroll
    for (int i = 0; i < N; ++i) {
        asm volatile("s_waitcnt vmcnt(%2)" : "+v"(kk[i]), "+v"(vv[i]) : "i"(2 * (N - 1 - i)) : "memory");
        lds_st16(lds + L_BK + (first + i) * 1024 + lane * 16, kk[i]);
        lds_st16(lds + L_BV + (first + i) * 1024 + lane * 16, vv[i]);
    }
}
DI void poll_b(const char* ws, int head, int jb, int lane, unsigned token) {
    const int tbk = lane & 15;
    poll_flags(gflag_addr(ws, 1, head, jb, lane), lane < 48 && tbk <= 2 * jb + 1 && tbk >= (lane < 32 ? 2 * jb - 2 : 2 * jb), token);
}

constexpr int G_STAGE = 28672, G_XB = 16384;

#define G_RD_X(NX, XA, XO)        "ds_read_b128 v[" #NX ":" #NX "+3], v" #XA " offset:" XO "\n\t"
#define G_RD_W(NW, WA, WO, C)     "ds_read_b64_tr_b16 v[" #NW ":" #NW "+1], v" #WA " offset:" WO "+" #C "*64\n\t" \
                                  "ds_read_b64_tr_b16 v[" #NW "+2:" #NW "+3], v" #WA " offset:" WO "+768+" #C "*64\n\t"
#define G_MF_T(ACC, X, W)         "v_mfma_f32_32x32x16_bf16 v[" #ACC ":" #ACC "+15], v[" #W ":" #W "+3], v[" #X ":" #X "+3], v[" #ACC ":" #ACC "+15]\n\t"
#define G_MF_C(ACC, X, W)         "v_mfma_f32_32x32x16_bf16 v[" #ACC ":" #ACC "+15], v[" #X ":" #X "+3], v[" #W ":" #W "+3], v[" #ACC ":" #ACC "+15]\n\t"
#define G_STEP(M0, M1, M2, PX, PW0, PW1, PW2, NX, NW0, NW1, NW2, XA, XO, WA, WO, PRE, POST) \
    PRE G_RD_X(NX, XA, XO) G_RD_W(NW0, WA, WO, 0) POST \
    M0(32, PX, PW0) G_RD_W(NW1, WA, WO, 1) M1(48, PX, PW1) G_RD_W(NW2, WA, WO, 2) M2(64, PX, PW2)
#define G_STEP_A(M0, M1, M2, XA, XO, WA, WO, PRE, POST) G_STEP(M0, M1, M2, 80, 84, 88, 92, 96, 100, 104, 108, XA, XO, WA, WO, PRE, POST)
#define G_STEP_B(M0, M1, M2, XA, XO, WA, WO, PRE, POST) G_STEP(M0, M1, M2, 96, 100, 104, 108, 80, 84, 88, 92, XA, XO, WA, WO, PRE, POST)
#define G_W3 "s_waitcnt lgkmcnt(3)\n\t"
#define G_STAGE_ASM(M0, M1, M2, X1, X2, X3, SO, WA, NX0, NSO, NWA) \
    G_STEP_A(M0, M1, M2, X1, SO, WA, SO "+16384+3072", "", G_W3) \
    G_STEP_B(M0, M1, M2, X2, SO, WA, SO "+16384+6144", "", G_W3) \
    G_STEP_A(M0, M1, M2, X3, SO, WA, SO "+16384+9216", "", G_W3) \
    G_STEP_B(M0, M1, M2, NX0, NSO, NWA, NSO "+16384", "s_waitcnt lgkmcnt(0)\n\ts_barrier\n\t", "")
#define G_ROUND_ASM(M0, M1, M2) \
    "s_mov_b32 %[cnt], 2\n\t" \
    "1:\n\t" \
    G_STAGE_ASM(M0, M1, M2, 113, 114, 115, "0", 120, 112, "28672", 120) \
    G_STAGE_ASM(M0, M1, M2, 113, 114, 115, "28672", 120, 116, "0", 121) \
    G_STAGE_ASM(M0, M1, M2, 117, 118, 119, "0", 121, 116, "28672", 121) \
    G_STAGE_ASM(M0, M1, M2, 117, 118, 119, "28672", 121, 112, "0", 120) \
    "s_sub_u32 %[cnt], %[cnt], 1\n\t" \
    "s_cmp_lg_u32 %[cnt], 0\n\t" \
    "s_cbranch_scc1 1b\n\t" \
    "s_waitcnt lgkmcnt(0)\n\t" \
    "s_nop 15\n\t"

struct GFrag { u32x4 x, w0, w1, w2; };
template <int VMASK>
DI void g_round(f32x16& a0, f32x16& a1, f32x16& a2, GFrag& f, u32x4 xa, u32x4 xb, unsigned wa, unsigned wb) {
    u32x4 t0, t1, t2, t3;
    int cnt;
#define G_ROUND_CALL(M0, M1, M2) \
    asm volatile(G_ROUND_ASM(M0, M1, M2) \
        : "+{v[32:47]}"(a0), "+{v[48:63]}"(a1), "+{v[64:79]}"(a2), "+{v[80:83]}"(f.x), "+{v[84:87]}"(f.w0), "+{v[88:91]}"(f.w1), "+{v[92:95]}"(f.w2), \
          "=&{v[96:99]}"(t0), "=&{v[100:103]}"(t1), "=&{v[104:107]}"(t2), "=&{v[108:111]}"(t3), [cnt] "=&s"(cnt) \
        : "{v[112:115]}"(xa), "{v[116:119]}"(xb), "{v120}"(wa), "{v121}"(wb) : "memory", "scc")
    if constexpr (VMASK == 0) G_ROUND_CALL(G_MF_T, G_MF_T, G_MF_T);
    else if constexpr (VMASK == 4) G_ROUND_CALL(G_MF_T, G_MF_T, G_MF_C);
    else G_ROUND_CALL(G_MF_C, G_MF_C, G_MF_C);
}
template <int VMASK>
DI void g_epilogue_piece(const f32x16& acc, int c, int u, char* ws, int br, int tb, int cb, int w, int lane) {
    const int t32 = lane & 31, h = lane >> 5, tile = tb * 4 + w;
    const int n0 = cb * 96 + c * 32, type = n0 >> 9, hd0 = (n0 & 511) >> 4;
    u32x4 v;
    if ((VMASK >> c) & 1) {
        char* dst = ws + WS_V + (size_t)br * TB + (size_t)((hd0 + (t32 >> 4)) * 64 + tile) * 1024 + h * 256 + (t32 & 15) * 16;
#pragma unroll
        for (int i = 0; i < 4; ++i) v[i] = cvtpk(acc[8 * u + 2 * i], acc[8 * u + 2 * i + 1]);
        st16_wt(dst + u * 512, v);
    } else {
        const float sc = type == 0 ? QSCALE : 1.0f;
        char* dst = ws + (type == 0 ? WS_Q : WS_K) + (size_t)br * TB + (size_t)(hd0 * 64 + tile) * 1024 + lane * 16;
#pragma unroll
        for (int i = 0; i < 4; ++i) v[i] = cvtpk(acc[8 * u + 2 * i] * sc, acc[8 * u + 2 * i + 1] * sc);
        st16_wt(dst + (size_t)u * 65536, v);
    }
}
template <int VMASK>
DI void g_epilogue_tile(const f32x16& acc, int c, char* ws, int br, int tb, int cb, int w, int lane) {
    g_epilogue_piece<VMASK>(acc, c, 0, ws, br, tb, cb, w, lane);
    g_epilogue_piece<VMASK>(acc, c, 1, ws, br, tb, cb, w, lane);
}
#define BAR() asm volatile("s_barrier" ::: "memory")
constexpr unsigned L_CTRL = 114688;
constexpr unsigned L_ASIG = 131056;
DI void publish4(LDSP char* ctr, char* flag, unsigned token, int lane) {
    asm volatile("s_waitcnt vmcnt(0)" ::: "memory");
    unsigned old = 0;
    if (lane == 0) old = __hip_atomic_fetch_add((LDSP unsigned*)ctr, 1u, __ATOMIC_RELAXED, __HIP_MEMORY_SCOPE_WORKGROUP);
    if (rfl(old) == 3 && lane < 8) st_flag(flag + lane * 2048, token);
}
DI void lds_signal(LDSP char* word, unsigned v) { *(volatile LDSP unsigned*)word = v; asm volatile("s_waitcnt lgkmcnt(0)" ::: "memory"); }
DI void lds_await(LDSP char* word, unsigned initial) {
    for (int it = 0; it < SPIN_MAX; ++it) {
        if (rfl(*(volatile LDSP unsigned*)word) != initial) break;
        __builtin_amdgcn_s_sleep(1);
    }
    asm volatile("" ::: "memory");
}
template <int VMASK>
DI u32x4 g_mfma_wave(char* ws, int br, int tb, int cb, int w, int lane, LDSP char* lds, unsigned token, bool fused) {
    const unsigned lds0 = (unsigned)(uintptr_t)lds;
    constexpr int PROFK = 0;
    const int w4 = w & 3, t32 = lane & 31, h = lane >> 5, f = ((t32 >> 1) & 7) ^ h;
    u32x4 xa, xb;
#pragma unroll
    for (int ks = 0; ks < 4; ++ks) { xa[ks] = lds0 + (unsigned)(w4 * 32 + t32) * 128u + ((f ^ (2 * ks)) * 16); xb[ks] = xa[ks] + 2 * G_STAGE; }
    const unsigned wa = lds0 + (unsigned)(8 * h + ((lane & 15) >> 2)) * 192u + (16 * ((lane >> 4) & 1) + 4 * (lane & 3)) * 2, wb = wa + 2 * G_STAGE;
    GFrag fr;
    BAR();
    if (br == 0) {
#pragma unroll
        for (int s = 0; s < 8; ++s) BAR();
    }
    STAMP(1);
    asm volatile("ds_read_b128 %0, %4\n\t"
                 "ds_read_b64_tr_b16 v[84:85], %5 offset:16384\n\tds_read_b64_tr_b16 v[86:87], %5 offset:16384+768\n\t"
                 "ds_read_b64_tr_b16 v[88:89], %5 offset:16384+64\n\tds_read_b64_tr_b16 v[90:91], %5 offset:16384+64+768\n\t"
                 "ds_read_b64_tr_b16 v[92:93], %5 offset:16384+128\n\tds_read_b64_tr_b16 v[94:95], %5 offset:16384+128+768\n\t"
                 "s_waitcnt lgkmcnt(0)"
                 : "=&{v[80:83]}"(fr.x), "=&{v[84:87]}"(fr.w0), "=&{v[88:91]}"(fr.w1), "=&{v[92:95]}"(fr.w2) : "v"(xa[0]), "v"(wa) : "memory");
    f32x16 acc[3] = {f32x16{}, f32x16{}, f32x16{}};
    g_round<VMASK>(acc[0], acc[1], acc[2], fr, xa, xb, wa, wb);
    STAMP(2);
    char* flag = ws + F_G + (br * 128 + cb) * 128 + tb * 4;
    const int bid = blockIdx.x, head = (bid & 7) * 4 + (bid >> 6), jb = (bid >> 3) & 7;
    const char* qp = ws + WS_Q + TB + (size_t)(head * 64 + jb * 8 + w) * 1024;
    u32x4 qb = {0, 0, 0, 0};
    if (br == 1) {
#define PLAN 1
        g_epilogue_tile<VMASK>(acc[0], 0, ws, br, tb, cb, w4, lane); BAR();
        g_epilogue_tile<VMASK>(acc[1], 1, ws, br, tb, cb, w4, lane); BAR();
        g_epilogue_tile<VMASK>(acc[2], 2, ws, br, tb, cb, w4, lane); BAR();
        STAMP(3);
        BAR();
        if (fused) publish4(lds + L_CTRL, flag, token, lane);
        BAR();
        unsigned pv = token;
        const int tbk = lane & 15;
        const bool want = w == 0 && lane < 48 && tbk <= 2 * jb + 1 && tbk >= (lane < 32 ? 2 * jb - 2 : 2 * jb);
        if (fused && want) asm volatile("global_load_dword %0, %1, off sc1" : "+v"(pv) : "v"(gflag_addr(ws, 1, head, jb, lane)) : "memory");
        BAR(); BAR(); BAR();
        if (fused) {
            STAMP(4);
            if (w == 0) {
                asm volatile("s_waitcnt vmcnt(0)" : "+v"(pv) :: "memory");
                if (!__all(pv == token)) poll_b(ws, head, jb, lane, token);
                lds_signal(lds + L_CTRL + 8, 1);
            } else lds_await(lds + L_CTRL + 8, 0);
            STAMP(5);
            qb = ld16_dev(qp, lane * 16);
            stage_window<4>(ws, lds, head, jb, w4 * 4, lane);
            asm volatile("s_waitcnt lgkmcnt(0)" : "+v"(qb) :: "memory");
            STAMP(6);
        }
        BAR();
    } else {
#pragma unroll
        for (int c = 0; c < 3; ++c) g_epilogue_tile<VMASK>(acc[c], c, ws, br, tb, cb, w4, lane);
        STAMP(3);
        if (fused) publish4(lds + L_CTRL + 4, flag, token, lane);
        STAMP(5);
        BAR();
    }
    return qb;
}
template <int NX>
DI void g_loader_wave(const char* xA, const char* wA, const char* xB, const char* wB, char* ws, int tb, int cb, int rot, int w, int lane, unsigned lds0) {
    constexpr int PROFK = 0;
    const int j = w & 3;
    unsigned go[NX], ll[NX];
#pragma unroll
    for (int i = 0; i < NX; ++i) {
        const int p = j + 4 * i;
        if (NX == 4) {
            const int row = 8 * p + (lane >> 3), sl = (lane & 7) ^ ((row >> 1) & 7);
            go[i] = (unsigned)(tb * 128 + row) * 1024u + sl * 16; ll[i] = p * 1024;
        } else {
            const int o = p * 1024 + lane * 16, k = o / 192;
            go[i] = (unsigned)k * 3072u + cb * 192 + (o - k * 192); ll[i] = G_XB + p * 1024;
        }
    }
    auto issue = [&](int s) {
        const int ks = (s + rot) & 7;
        const char* src = NX == 4 ? (s < 8 ? xB : xA) + ks * 128 : (s < 8 ? wB : wA) + (size_t)ks * 64 * 3072;
        const unsigned slot = lds0 + (s & 3) * G_STAGE;
#pragma unroll
        for (int i = 0; i < NX; ++i) glds16(src, go[i], slot + ll[i]);
    };
    issue(0); issue(1);
    if (NX == 4) WAITV_BAR(4); else WAITV_BAR(3);
    issue(2); issue(3);
#pragma unroll
    for (int s = 0; s < 16; ++s) {
        if (s <= 12) { if (NX == 4) WAITV_BAR(8); else WAITV_BAR(6); }
        else if (s == 13) { if (NX == 4) WAITV_BAR(4); else WAITV_BAR(3); }
        else WAITV_BAR(0);
        if (w == 15) STAMPX(s + 1);
        if (s + 4 < 16) issue(s + 4);
    }
    BAR();
}
DI u32x4 phase_qkv(const char* xA, const char* wA, const char* xB, const char* wB, char* ws, LDSP char* lds, unsigned token, bool fused) {
    constexpr int PROFK = 0;
    const int lane = threadIdx.x & 63, w = rfl(threadIdx.x >> 6);
    const int bid = blockIdx.x, xcd = bid & 7, idx = bid >> 3;
    const int tb = (xcd & 3) * 4 + (idx & 3), cb = (xcd >> 2) * 8 + (idx >> 2);
    const int rot = (idx >> 2) + 2 * (idx & 3);
    const unsigned lds0 = (unsigned)(uintptr_t)lds;
    STAMP(0);
    if (w == 15) STAMPX(0);
    u32x4 qb = {0, 0, 0, 0};
    if (threadIdx.x == 0) {
        *(LDSP unsigned*)(lds + L_CTRL) = 0; *(LDSP unsigned*)(lds + L_CTRL + 4) = 0; *(LDSP unsigned*)(lds + L_CTRL + 8) = 0;
        *(LDSP unsigned*)(lds + L_ASIG) = ~token;
        asm volatile("s_waitcnt lgkmcnt(0)" ::: "memory");
    }
    if (w < 8) {
        const int br = w < 4 ? 1 : 0;
        if (cb < 10) qb = g_mfma_wave<0>(ws, br, tb, cb, w, lane, lds, token, fused);
        else if (cb == 10) qb = g_mfma_wave<4>(ws, br, tb, cb, w, lane, lds, token, fused);
        else qb = g_mfma_wave<7>(ws, br, tb, cb, w, lane, lds, token, fused);
    } else if (w < 12) g_loader_wave<4>(xA, wA, xB, wB, ws, tb, cb, rot, w, lane, lds0);
    else g_loader_wave<3>(xA, wA, xB, wB, ws, tb, cb, rot, w, lane, lds0);
    STAMP(7);
    return qb;
}

constexpr float SAFE_L = 1.2676506e30f;
constexpr float SAFE_O = 1.3292280e36f;
constexpr float NEG_BIG = -3.0e38f;
constexpr unsigned ONES = 0x3F803F80u;

DI float xhalf_max(float m) {
    auto r = __builtin_amdgcn_permlane32_swap(__float_as_uint(m), __float_as_uint(m), false, false);
    return fmaxf(__uint_as_float(r[0]), __uint_as_float(r[1]));
}
DI float max16(const f32x16& s) {
    float a = fmaxf(fmaxf(s[0], s[1]), s[2]), b = fmaxf(fmaxf(s[3], s[4]), s[5]);
    a = fmaxf(fmaxf(a, s[6]), s[7]); b = fmaxf(fmaxf(b, s[8]), s[9]);
    a = fmaxf(fmaxf(a, s[10]), s[11]); b = fmaxf(fmaxf(b, s[12]), s[13]);
    a = fmaxf(fmaxf(a, s[14]), s[15]);
    return fmaxf(a, b);
}
DI f32x16 bcast16(float v) { f32x16 r; for (int i = 0; i < 16; ++i) r[i] = v; return r; }
DI u32x4 v_init(int lane) { const unsigned x = ((lane & 31) == 16 || (lane & 31) == 20) ? ONES : 0u; return u32x4{x, x, x, x}; }
DI void load_v(u32x4& v0, u32x4& v1, unsigned addr) {
    asm volatile("s_mov_b64 exec, %3\n\tds_read_b128 %0, %2\n\tds_read_b128 %1, %2 offset:512\n\ts_mov_b64 exec, -1\n\ts_waitcnt lgkmcnt(0)"
                 : "+v"(v0), "+v"(v1) : "v"(addr), "s"(0x0000FFFF0000FFFFull) : "memory");
}
DI void pv_regs(const f32x16& p, f32x16& o, const u32x4& v0, const u32x4& v1) {
    u32x4 p0, p1;
#pragma unroll
    for (int i = 0; i < 4; ++i) { p0[i] = cvtpk(p[2 * i], p[2 * i + 1]); p1[i] = cvtpk(p[8 + 2 * i], p[9 + 2 * i]); }
    o = mfma(__builtin_bit_cast(bf16x8, v0), __builtin_bit_cast(bf16x8, p0), o);
    o = mfma(__builtin_bit_cast(bf16x8, v1), __builtin_bit_cast(bf16x8, p1), o);
}
DI void mask_tile(f32x16& s, int mode, int z) {
    if (mode == 0) return;
#pragma unroll
    for (int r = 0; r < 16; ++r) {
        const int cr = (r & 3) + 8 * (r >> 2);
        const bool keep = mode == 1 ? (cr <= z) : (cr > z);
        s[r] = keep ? s[r] : -INFINITY;
    }
}
DI bool unsafe_state(const f32x16& o) {
    float mx = fabsf(o[0]);
#pragma unroll
    for (int i = 1; i < 8; ++i) mx = fmaxf(mx, fabsf(o[i]));
    bool nan = false;
#pragma unroll
    for (int i = 0; i < 9; ++i) nan |= !(o[i] == o[i]);
    return nan || !(mx < SAFE_O) || !(o[8] < SAFE_L);
}
DI void safe_tiles(const char* Kg, const char* Vg, int t0, int n, int step, int first_mode, int last_mode, bf16x8 q, int lane, f32x16& o, float& m) {
    const int z = (lane & 31) - 4 * (lane >> 5);
    const unsigned vl = (lane >> 5) * 256 + (lane & 15) * 16;
    u32x4 v0 = v_init(lane), v1 = v0;
    o = f32x16{}; m = NEG_BIG;
    for (int i = 0; i < n; ++i) {
        const int t = t0 + i * step;
        u32x4 kr = ld16_dev(Kg + (size_t)t * 1024, lane * 16), a0 = ld16_dev(Vg + (size_t)t * 1024, vl), a1 = ld16_dev(Vg + (size_t)t * 1024, vl + 512);
        asm volatile("s_waitcnt vmcnt(0)" : "+v"(kr), "+v"(a0), "+v"(a1) :: "memory");
        const bf16x8 kf = __builtin_bit_cast(bf16x8, kr);
        if ((lane & 16) == 0) { v0 = a0; v1 = a1; }
        f32x16 s = mfma(kf, q, f32x16{});
        mask_tile(s, i == 0 ? first_mode : (i == n - 1 ? last_mode : 0), z);
        const float mn = fmaxf(m, xhalf_max(max16(s)));
        const float al = __builtin_amdgcn_exp2f(m - mn);
        m = mn;
#pragma unroll
        for (int r = 0; r < 16; ++r) { o[r] *= al; s[r] = __builtin_amdgcn_exp2f(s[r] - mn); }
        pv_regs(s, o, v0, v1);
    }
}

#define T_EXP(C, i) "v_exp_f32 v[" #C "+" #i "], v[" #C "+" #i "]\n\t"
#define T_CVT(C, d, a, b) "v_cvt_pk_bf16_f32 v[" #C "+" #d "], v[" #C "+" #a "], v[" #C "+" #b "]\n\t"
#define T_WAIT(n) "s_waitcnt lgkmcnt(" #n ")\n\t"
#define T_QK(N, CR) "v_mfma_f32_32x32x16_bf16 v[" #N ":" #N "+15], v[68:71], v[64:67], v[" #CR ":" #CR "+15]\n\t"
#define T_KRD(KOFF) "ds_read_b128 v[68:71], v30 offset:" #KOFF "\n\t"
#define X_TILE(C, PV1P, QKN, W0, VRD) \
    T_EXP(C, 0) T_EXP(C, 1) T_EXP(C, 2) T_EXP(C, 3) \
    PV1P \
    T_EXP(C, 4) T_EXP(C, 5) T_EXP(C, 6) T_EXP(C, 7) T_EXP(C, 8) \
    QKN \
    T_CVT(C, 0, 0, 1) T_CVT(C, 1, 2, 3) T_CVT(C, 2, 4, 5) T_CVT(C, 3, 6, 7) T_EXP(C, 9) T_EXP(C, 10) \
    W0 \
    "v_mfma_f32_32x32x16_bf16 v[96:111], v[72:75], v[" #C ":" #C "+3], v[96:111]\n\t" \
    VRD \
    T_EXP(C, 11) T_EXP(C, 12) T_EXP(C, 13) T_EXP(C, 14) T_EXP(C, 15) T_CVT(C, 4, 8, 9) T_CVT(C, 5, 10, 11) T_CVT(C, 6, 12, 13) T_CVT(C, 7, 14, 15)
#define T_PV1(P) "v_mfma_f32_32x32x16_bf16 v[96:111], v[76:79], v[" #P "+4:" #P "+7], v[96:111]\n\t"
#define T_XON "s_mov_b64 exec, %[msk]\n\t"
#define T_XOFF "s_mov_b64 exec, -1\n\t"
#define T_V0(O) "ds_read_b128 v[72:75], v31 offset:" #O "\n\t"
#define T_V1(O) "ds_read_b128 v[76:79], v31 offset:" #O "+512\n\t"
#define A_TILE(C, N, KOFF, V1OFF, V0OFF) X_TILE(C, T_WAIT(1) T_PV1(N), T_QK(N, 80) T_KRD(KOFF), T_WAIT(1), T_XON T_V1(V1OFF) T_V0(V0OFF) T_XOFF)
#define A_TILE_FIRST(C, N, KOFF, V1OFF, V0OFF) X_TILE(C, "", T_WAIT(1) T_QK(N, 80) T_KRD(KOFF), T_WAIT(1), T_XON T_V1(V1OFF) T_V0(V0OFF) T_XOFF)
#define T_DRAIN(P) T_WAIT(0) "s_nop 1\n\t" T_PV1(P) "s_nop 15\n\t"

DI void run_b9(unsigned kaddr, unsigned vaddr, bf16x8 q, f32x16 s0, const f32x16& nm, const f32x16& cfar, f32x16& o, u32x4 v0) {
    u32x4 v1 = v0, kf;
    f32x16 s1;
    asm volatile(
        T_KRD(7168) T_XON T_V0(8192) T_XOFF
        A_TILE_FIRST(32, 48, 6144, 8192, 7168)
        A_TILE(48, 32, 5120, 7168, 6144)
        A_TILE(32, 48, 4096, 6144, 5120)
        A_TILE(48, 32, 3072, 5120, 4096)
        A_TILE(32, 48, 2048, 4096, 3072)
        A_TILE(48, 32, 1024, 3072, 2048)
        A_TILE(32, 48, 0, 2048, 1024)
        X_TILE(48, T_WAIT(1) T_PV1(32), T_QK(32, 112), T_WAIT(0), T_XON T_V1(1024) T_V0(0) T_XOFF)
        X_TILE(32, T_WAIT(1) T_PV1(48), "", T_WAIT(0), T_XON T_V1(0) T_XOFF)
        T_DRAIN(32)
        : "+{v[32:47]}"(s0), "=&{v[48:63]}"(s1), "=&{v[68:71]}"(kf), "+{v[72:75]}"(v0), "+{v[76:79]}"(v1), "+{v[96:111]}"(o)
        : "{v30}"(kaddr), "{v31}"(vaddr), "{v[64:67]}"(q), "{v[80:95]}"(nm), "{v[112:127]}"(cfar), [msk] "s"(0x0000FFFF0000FFFFull)
        : "memory");
}

#define A_PAIR(K0) A_TILE(48, 32, K0+2048, K0, K0+1024) A_TILE(32, 48, K0+3072, K0+1024, K0+2048)
DI void run_a(unsigned kaddr, unsigned vaddr, bf16x8 q, const f32x16& nm, f32x16& o, u32x4 v0) {
    u32x4 v1 = v0;
    f32x16 s0, s1;
    u32x4 kf;
    asm volatile(
        "ds_read_b128 v[68:71], v30\n\t"
        "s_waitcnt lgkmcnt(0)\n\t"
        "v_mfma_f32_32x32x16_bf16 v[32:47], v[68:71], v[64:67], v[80:95]\n\t"
        T_KRD(1024) T_XON T_V0(0) T_XOFF
        "s_nop 7\n\t"
        "s_nop 3\n\t"
        A_TILE_FIRST(32, 48, 2048, 0, 1024)
        A_PAIR(1024) A_PAIR(3072) A_PAIR(5120) A_PAIR(7168) A_PAIR(9216) A_PAIR(11264) A_PAIR(13312) A_PAIR(15360)
        A_PAIR(17408) A_PAIR(19456) A_PAIR(21504) A_PAIR(23552) A_PAIR(25600) A_PAIR(27648) A_PAIR(29696)
        X_TILE(48, T_WAIT(1) T_PV1(32), "", T_WAIT(0), T_XON T_V1(31744) T_XOFF)
        T_DRAIN(48)
        : "=&{v[32:47]}"(s0), "=&{v[48:63]}"(s1), "=&{v[68:71]}"(kf), "+{v[72:75]}"(v0), "+{v[76:79]}"(v1), "+{v[96:111]}"(o)
        : "{v30}"(kaddr), "{v31}"(vaddr), "{v[64:67]}"(q), "{v[80:95]}"(nm), [msk] "s"(0x0000FFFF0000FFFFull)
        : "memory");
}

DI void store_o(char* ws, int br, int qtile, int head, int lane, const f32x16& o) {
    const float inv = 1.0f / o[8];
    u32x4 v;
#pragma unroll
    for (int i = 0; i < 4; ++i) v[i] = cvtpk(o[2 * i] * inv, o[2 * i + 1] * inv);
    st16_wt(ws + WS_O + (size_t)br * TB + (size_t)(qtile * 32 + head) * 1024 + lane * 16, v);
}
DI void put_partial(LDSP float* ex, const f32x16& o, float m) {
#pragma unroll
    for (int r = 0; r < 9; ++r) ex[r * 64] = o[r];
    ex[9 * 64] = m;
}
DI void merge_partial(const LDSP float* ex, f32x16& o, float m) {
    const float m1 = ex[9 * 64], mn = fmaxf(m, m1);
    const float s0 = __builtin_amdgcn_exp2f(m - mn), s1 = __builtin_amdgcn_exp2f(m1 - mn);
#pragma unroll
    for (int r = 0; r < 9; ++r) o[r] = o[r] * s0 + ex[r * 64] * s1;
}
DI constexpr unsigned wo_piece(int p) { return p < 32 ? L_BK + p * 1024 : L_AK + p * 1024; }
DI void wo_issue(const char* Wo, int nb, int p, int lane, unsigned lds0) {
    const int r = lane >> 3, sl = (lane & 7) ^ (((r >> 1) & 1) << 2);
    glds16(Wo + (size_t)p * 8192, (unsigned)r * 1024u + nb * 128 + sl * 16, lds0 + (p < 32 ? L_BK : L_AK) + p * 1024);
}

DI void poll_a(const char* ws, int head, int jb, int lane, unsigned token) {
    poll_flags(gflag_addr(ws, 0, head, jb, lane), lane < 32 || (lane < 48 && ((lane & 15) >> 1) == jb), token);
}

template <bool FUSED>
DI void phase_attn(char* ws, const char* Wo, int nb, LDSP char* lds, unsigned token, u32x4 qbr) {
    constexpr int PROFK = 1;
    const unsigned lds0 = (unsigned)(uintptr_t)lds;
    int lane = fresh_lane();
    const int w = rfl(threadIdx.x >> 6);
    const int bid = blockIdx.x, head = (bid & 7) * 4 + (bid >> 6), jb = (bid >> 3) & 7;
    const int qt = w & 7, half = w >> 3, Qt = jb * 8 + qt;
    const char* KA = ws + WS_K + (size_t)head * 65536;
    const char* VA = ws + WS_V + (size_t)head * 65536;
    const char* qp = ws + WS_Q + (size_t)(head * 64 + Qt) * 1024;
    const unsigned lo = lane * 16;
    const int z = (lane & 31) - 4 * (lane >> 5);
    const unsigned vl = (lane >> 5) * 256 + (lane & 15) * 16;
    const u32x4 vinit = v_init(lane);
    STAMP(0);
    if (!FUSED) {
        stage_window<1>(ws, lds, head, jb, w, lane);
        asm volatile("s_waitcnt lgkmcnt(0)\n\ts_barrier" ::: "memory");
    }
    u32x4 qar;
    if (half == 0) {
        if (!FUSED || w >= 4) { qbr = ld16_dev(qp + TB, lo); asm volatile("s_waitcnt vmcnt(0)" : "+v"(qbr) :: "memory"); }
        const bf16x8 qb = __builtin_bit_cast(bf16x8, qbr);
        STAMP(1);
        const int t0 = qt + 8, nt = jb ? 9 : qt + 1;
        f32x16 o = f32x16{};
        f32x16 s = mfma(lds_frag(lds + L_BK + t0 * 1024 + lo), qb, f32x16{});
        mask_tile(s, 1, z);
        float m = max16(s);
        if (nt > 1) m = fmaxf(m, max16(mfma(lds_frag(lds + L_BK + (t0 - 1) * 1024 + lo), qb, f32x16{})));
        m = xhalf_max(m);
        const f32x16 nm = bcast16(-m);
        if (jb) {
#pragma unroll
            for (int r = 0; r < 16; ++r) s[r] -= m;
            f32x16 cfar = nm;
            mask_tile(cfar, 2, z);
            run_b9(lds0 + L_BK + qt * 1024 + lo, lds0 + L_BV + qt * 1024 + vl, qb, s, nm, cfar, o, vinit);
        } else {
            u32x4 v0 = vinit, v1 = vinit;
            for (int i = 0; i < nt; ++i) {
                f32x16 p = mfma(lds_frag(lds + L_BK + (t0 - i) * 1024 + lo), qb, nm);
                if (i == 0) mask_tile(p, 1, z);
#pragma unroll
                for (int r = 0; r < 16; ++r) p[r] = __builtin_amdgcn_exp2f(p[r]);
                load_v(v0, v1, lds0 + L_BV + (t0 - i) * 1024 + vl);
                pv_regs(p, o, v0, v1);
            }
        }
        lane = fresh_lane();
        if (__any(unsafe_state(o)))
            safe_tiles(ws + WS_K + TB + (size_t)head * 65536, ws + WS_V + TB + (size_t)head * 65536, Qt, nt, -1, 1, jb ? 2 : 0, qb, lane, o, m);
        store_o(ws, 1, Qt, head, lane, o);
        asm volatile("s_waitcnt vmcnt(0)" ::: "memory");
    } else {
        if (FUSED) {
            if (w == 8) { poll_a(ws, head, jb, lane, token); lds_signal(lds + L_ASIG, token); }
            else lds_await(lds + L_ASIG, ~token);
        }
        STAMP(1);
        u32x4 sk[8], sv[8];
        qar = ld16_dev(qp, lo);
#pragma unroll
        for (int i = 0; i < 8; ++i) { sk[i] = ld16_dev(KA + (size_t)(qt + 8 * i) * 1024, lo); sv[i] = ld16_dev(VA + (size_t)(qt + 8 * i) * 1024, lo); }
#pragma unroll
        for (int i = 0; i < 8; ++i) {
            asm volatile("s_waitcnt vmcnt(%3)" : "+v"(sk[i]), "+v"(sv[i]), "+v"(qar) : "i"(2 * (7 - i)) : "memory");
            lds_st16(lds + L_AK + (qt + 8 * i) * 1024 + lo, sk[i]);
            lds_st16(lds + L_AV + (qt + 8 * i) * 1024 + lo, sv[i]);
        }
    }
    STAMP(2);
    asm volatile("s_waitcnt lgkmcnt(0)\n\ts_barrier" ::: "memory");
    STAMP(3);
    if (FUSED && threadIdx.x < 4) st_flag(ws + F_O + ((4 + threadIdx.x) * 8 + jb) * 128 + head * 4, token);
    if (w >= 12) {
        lane = fresh_lane();
#pragma unroll
        for (int i = 0; i < 8; ++i) wo_issue(Wo, nb, (w - 12) * 8 + i, lane, lds0);
    }
    lane = fresh_lane();
    if (half == 0) { qar = ld16_dev(qp, lane * 16); asm volatile("s_waitcnt vmcnt(0)" : "+v"(qar) :: "memory"); }
    const bf16x8 qa = __builtin_bit_cast(bf16x8, qar);
    f32x16 oa = f32x16{};
    float ma = xhalf_max(max16(mfma(lds_frag(lds + L_AK + half * 32768 + lane * 16), qa, f32x16{})));
    const f32x16 nma = bcast16(-ma);
    run_a(lds0 + L_AK + half * 32768 + lane * 16, lds0 + L_AV + half * 32768 + (lane >> 5) * 256 + (lane & 15) * 16, qa, nma, oa, v_init(lane));
    lane = fresh_lane();
    STAMP(4);
    if (__any(unsafe_state(oa))) safe_tiles(KA, VA, half * 32, 32, 1, 0, 0, qa, lane, oa, ma);
    asm volatile("s_waitcnt lgkmcnt(0)\n\ts_barrier" ::: "memory");
    STAMP(5);
    LDSP float* ex = (LDSP float*)(lds + L_AK) + qt * 640 + lane;
    if (half == 1) {
        put_partial(ex, oa, ma);
#pragma unroll
        for (int i = 0; i < 4; ++i) wo_issue(Wo, nb, 32 + (w - 8) * 4 + i, lane, lds0);
    }
    asm volatile("s_waitcnt lgkmcnt(0)\n\ts_barrier" ::: "memory");
    if (half == 0) { merge_partial(ex, oa, ma); store_o(ws, 0, Qt, head, lane, oa); }
    asm volatile("s_waitcnt vmcnt(0)\n\ts_barrier" ::: "memory");
    if (FUSED && threadIdx.x < 4) st_flag(ws + F_O + (threadIdx.x * 8 + jb) * 128 + head * 4, token);
    STAMP(6);
}

template <bool FUSED>
DI void phase_out(char* ws, char* out, int br, int qb, int nb, LDSP char* lds, unsigned token) {
    constexpr int PROFK = 2;
    const int lane = threadIdx.x & 63, w = rfl(threadIdx.x >> 6);
    STAMP(0);
    if (FUSED) {
        if (w == 0) poll_flags(ws + F_O + ((br * 4 + (nb & 3)) * 8 + (qb >> 1)) * 128 + (lane & 31) * 4, lane < 32, token);
        BAR();
    }
    STAMP(1);
    const int qt = w & 3, kh = (w >> 2) & 1;
    f32x16 acc[2] = {f32x16{}, f32x16{}};
    if (w < 8) {
        const char* Of = ws + WS_O + (size_t)br * TB + (size_t)(qb * 4 + qt) * 32768 + kh * 16384;
        u32x4 orr[16];
#pragma unroll
        for (int i = 0; i < 16; ++i) orr[i] = ld16_dev(Of + i * 1024, lane * 16);
        asm volatile("s_waitcnt vmcnt(0)" : "+v"(orr[0]), "+v"(orr[1]), "+v"(orr[2]), "+v"(orr[3]), "+v"(orr[4]), "+v"(orr[5]), "+v"(orr[6]), "+v"(orr[7]),
                     "+v"(orr[8]), "+v"(orr[9]), "+v"(orr[10]), "+v"(orr[11]), "+v"(orr[12]), "+v"(orr[13]), "+v"(orr[14]), "+v"(orr[15]) :: "memory");
        bf16x8 of[16];
#pragma unroll
        for (int i = 0; i < 16; ++i) of[i] = __builtin_bit_cast(bf16x8, orr[i]);
        const int h = lane >> 5, g1 = (lane >> 4) & 1, q = (lane & 15) >> 2, p = lane & 3;
        unsigned wa[2];
#pragma unroll
        for (int nt = 0; nt < 2; ++nt) {
            const int colb = (nt * 32 + 16 * (p & 1) + 8 * g1 + 4 * (p >> 1)) * 2;
            wa[nt] = (unsigned)(4 * h + q) * 128u + (colb ^ (((q >> 1) & 1) << 6));
        }
        auto body = [&](auto KH) {
            constexpr int kh_ = decltype(KH)::value;
#pragma unroll
            for (int i = 0; i < 16; ++i)
#pragma unroll
                for (int nt = 0; nt < 2; ++nt) {
                    const unsigned base = wo_piece(2 * (kh_ * 16 + i));
                    const bf16x8 wf = cat44(lds_tr(lds + wa[nt] + base), lds_tr(lds + wa[nt] + base + 1024));
                    acc[nt] = mfma(wf, of[i], acc[nt]);
                }
        };
        if (kh == 0) body(std::integral_constant<int, 0>{}); else body(std::integral_constant<int, 1>{});
    }
    STAMP(2);
    LDSP f32x4* ex = (LDSP f32x4*)(lds + L_AV + qt * 8192) + lane;
    if (w >= 4 && w < 8) {
#pragma unroll
        for (int nt = 0; nt < 2; ++nt)
#pragma unroll
            for (int i = 0; i < 4; ++i) ex[(nt * 4 + i) * 64] = f32x4{acc[nt][4 * i], acc[nt][4 * i + 1], acc[nt][4 * i + 2], acc[nt][4 * i + 3]};
    }
    asm volatile("s_waitcnt lgkmcnt(0)\n\ts_barrier" ::: "memory");
    if (w < 4) {
        const int h = lane >> 5;
        char* dst = out + (size_t)((qb * 4 + qt) * 32 + (lane & 31)) * 2048 + br * 1024 + nb * 128 + h * 32;
#pragma unroll
        for (int nt = 0; nt < 2; ++nt) {
#pragma unroll
            for (int i = 0; i < 4; ++i) { const f32x4 t = ex[(nt * 4 + i) * 64]; for (int e = 0; e < 4; ++e) acc[nt][4 * i + e] += t[e]; }
#pragma unroll
            for (int u = 0; u < 2; ++u) {
                u32x4 v;
#pragma unroll
                for (int i = 0; i < 4; ++i) v[i] = cvtpk(acc[nt][8 * u + 2 * i], acc[nt][8 * u + 2 * i + 1]);
                *(u32x4*)(dst + nt * 64 + u * 16) = v;
            }
        }
    }
    STAMP(3);
}

template <int PH>
__global__ __launch_bounds__(1024) void k_main(const char* xA, const char* wA, const char* oA, const char* xB, const char* wB, const char* oB, char* ws, char* out) {
    extern __shared__ __attribute__((aligned(16))) char smem[];
    LDSP char* lds = (LDSP char*)smem;
    constexpr bool FUSED = PH == 7;
    const int bid = blockIdx.x, xcd = bid & 7, idx = bid >> 3;
    const int br = xcd >> 2, qb = (xcd & 3) * 4 + (idx & 3), nb = idx >> 2;
    unsigned token = 0;
    { constexpr int PROFK = PH == 7 ? 0 : (PH == 1 ? 0 : PH == 2 ? 1 : 2); RSTAMP(0); }
    if (FUSED) token = rfl(ld_flag(ws + F_EPOCH)) + 1;
    u32x4 qfrag = {0, 0, 0, 0};
    if (PH & 1) qfrag = phase_qkv(xA, wA, xB, wB, ws, lds, token, FUSED);
    if (PH & 2) phase_attn<FUSED>(ws, br ? oB : oA, nb, lds, token, qfrag);
    if (PH == 4) {
        const int lane = threadIdx.x & 63, w = rfl(threadIdx.x >> 6);
        for (int i = 0; i < 4; ++i) wo_issue(br ? oB : oA, nb, i * 16 + w, lane, (unsigned)(uintptr_t)lds);
        asm volatile("s_waitcnt vmcnt(0)\n\ts_barrier" ::: "memory");
    }
    if (PH & 4) phase_out<FUSED>(ws, out, br, qb, nb, lds, token);
    if (FUSED && bid == 0 && threadIdx.x < 64) {
        const int lane = threadIdx.x;
#pragma unroll
        for (int i = 0; i < 4; ++i) poll_flags(ws + F_G + (i * 4 + (lane >> 4)) * 128 + (lane & 15) * 4, true, token);
        if (lane == 0) st_flag(ws + F_EPOCH, token);
    }
    { constexpr int PROFK = PH == 7 ? 0 : (PH == 1 ? 0 : PH == 2 ? 1 : 2); RSTAMP(1); }
}

extern "C" void kernel_launch(void* const* d_in, const int* in_sizes, int n_in, void* d_out, int out_size, void* d_ws, size_t ws_size,
                              hipStream_t stream) {
    const char* xA = (const char*)d_in[0];
    const char* wA = (const char*)d_in[1];
    const char* oA = (const char*)d_in[2];
    const char* xB = (const char*)d_in[3];
    const char* wB = (const char*)d_in[4];
    const char* oB = (const char*)d_in[5];
    char* ws = (char*)d_ws;
    char* out = (char*)d_out;
    (void)hipFuncSetAttribute((const void*)k_main<7>, hipFuncAttributeMaxDynamicSharedMemorySize, 163840);
    k_main<7><<<256, 1024, 163840, stream>>>(xA, wA, oA, xB, wB, oB, ws, out);
}
```

```cpp
#include <hip/hip_runtime.h>
#include <hip/hip_bf16.h>
#include <stdint.h>
#include <type_traits>

typedef __bf16 bf16x8 __attribute__((ext_vector_type(8)));
typedef __bf16 bf16x2 __attribute__((ext_vector_type(2)));
typedef short s16x4 __attribute__((ext_vector_type(4)));
typedef short s16x8 __attribute__((ext_vector_type(8)));
typedef float f32x16 __attribute__((ext_vector_type(16)));
typedef float f32x4 __attribute__((ext_vector_type(4)));
typedef float f32x2 __attribute__((ext_vector_type(2)));
typedef unsigned u32x4 __attribute__((ext_vector_type(4)));
#define LDSP __attribute__((address_space(3)))
#define DI __device__ __forceinline__

constexpr size_t TB = 2u << 20;
constexpr size_t WS_Q = 0, WS_K = 2 * TB, WS_V = 4 * TB, WS_O = 6 * TB;
constexpr float QSCALE = 0.25f * 1.4426950408889634f;

DI unsigned cvtpk(float lo, float hi) {
    f32x2 v = {lo, hi};
    bf16x2 b = __builtin_convertvector(v, bf16x2);
    return __builtin_bit_cast(unsigned, b);
}
DI f32x16 mfma(bf16x8 a, bf16x8 b, f32x16 c) { return __builtin_amdgcn_mfma_f32_32x32x16_bf16(a, b, c, 0, 0, 0); }
DI int rfl(int x) { return __builtin_amdgcn_readfirstlane(x); }
DI const char* uni(const void* p) {
    const uint64_t a = (uint64_t)p;
    return (const char*)(((uint64_t)(unsigned)rfl((int)(a >> 32)) << 32) | (unsigned)rfl((int)a));
}
DI void glds16(const void* sbase, unsigned voff, unsigned lds_base) {
    unsigned sv;
    asm volatile("s_mov_b32 %0, m0\n\ts_mov_b32 m0, %3\n\ts_nop 2\n\tglobal_load_lds_dwordx4 %1, %2\n\ts_mov_b32 m0, %0"
                 : "=&s"(sv) : "v"(voff), "s"(uni(sbase)), "s"(rfl((int)lds_base)) : "memory");
}
DI void glds16_dev(const void* sbase, unsigned voff, unsigned lds_base) {
    unsigned sv;
    asm volatile("s_mov_b32 %0, m0\n\ts_mov_b32 m0, %3\n\ts_nop 2\n\tglobal_load_lds_dwordx4 %1, %2 sc1\n\ts_mov_b32 m0, %0"
                 : "=&s"(sv) : "v"(voff), "s"(uni(sbase)), "s"(rfl((int)lds_base)) : "memory");
}
DI int fresh_lane() { int l; asm volatile("v_mbcnt_lo_u32_b32 %0, -1, 0\n\tv_mbcnt_hi_u32_b32 %0, -1, %0" : "=v"(l)); return l; }
DI bf16x8 lds_frag(const LDSP char* p) { return *(const LDSP bf16x8*)p; }
DI s16x4 lds_tr(const LDSP char* p) { return __builtin_amdgcn_ds_read_tr16_b64_v4i16((LDSP s16x4*)p); }
DI bf16x8 cat44(s16x4 a, s16x4 b) {
    s16x8 r = {a[0], a[1], a[2], a[3], b[0], b[1], b[2], b[3]};
    return __builtin_bit_cast(bf16x8, r);
}
#define STAMP(i)
#define RSTAMP(i)
#define STAMPX(i)
#define WAIT_BAR(N) asm volatile("s_waitcnt vmcnt(" #N ") lgkmcnt(0)\n\ts_barrier" ::: "memory")
#define WAITV_BAR(N) asm volatile("s_waitcnt vmcnt(" #N ")\n\ts_barrier" ::: "memory")

constexpr size_t WS_F = 8 * TB;
constexpr size_t F_EPOCH = WS_F, F_G = WS_F + 4096, F_O = F_G + 32768;
constexpr int SPIN_MAX = 1 << 18;
typedef __attribute__((address_space(1))) unsigned gu32;
DI unsigned ld_flag(const char* p) { return __hip_atomic_load((gu32*)p, __ATOMIC_RELAXED, __HIP_MEMORY_SCOPE_AGENT); }
DI void st_flag(char* p, unsigned v) { __hip_atomic_store((gu32*)p, v, __ATOMIC_RELAXED, __HIP_MEMORY_SCOPE_AGENT); }
DI void st16_wt(char* p, u32x4 v) { asm volatile("global_store_dwordx4 %0, %1, off sc1\n\ts_nop 1" :: "v"(p), "v"(v) : "memory"); }
DI void acquire_agent() {
    __builtin_amdgcn_fence(__ATOMIC_ACQUIRE, "agent");
    asm volatile("s_waitcnt vmcnt(0)" ::: "memory");
}
DI u32x4 ld16_dev(const char* sbase, unsigned voff) {
    u32x4 r;
    asm volatile("s_nop 4\n\tglobal_load_dwordx4 %0, %1, %2 sc1" : "=&v"(r) : "v"(voff), "s"(uni(sbase)) : "memory");
    return r;
}
DI void poll_flags(const char* p, bool want, unsigned token) {
#pragma unroll 1
    for (int it = 0; it < SPIN_MAX; ++it) {
        const unsigned v = want ? ld_flag(p) : token;
        if (__all(v == token)) break;
        __builtin_amdgcn_s_sleep(4);
    }
}
DI const char* gflag_addr(const char* ws, int br, int head, int jb, int lane) {
    const int type = lane < 16 ? 1 : (lane < 32 ? 2 : 0), cb = (type * 512 + head * 16) / 96;
    return ws + F_G + ((br * 8 + jb) * 16 + cb) * 128 + (lane & 15) * 4;
}

constexpr unsigned L_AK = 0, L_AV = 65536, L_BK = 131072, L_BV = 147456;
DI void lds_st16(LDSP char* p, u32x4 v) { *(LDSP u32x4*)p = v; }
template <int N>
DI void stage_window(const char* ws, unsigned lds0, int head, int jb, int first, int lane) {
    const char* KB = ws + WS_K + TB + (size_t)head * 65536;
    const char* VB = ws + WS_V + TB + (size_t)head * 65536;
#pragma unroll
    for (int i = 0; i < N; ++i) {
        int t = jb * 8 - 8 + first + i; if (t < 0) t = 0;
        glds16_dev(KB + (size_t)t * 1024, lane * 16, lds0 + L_BK + (first + i) * 1024);
        glds16_dev(VB + (size_t)t * 1024, lane * 16, lds0 + L_BV + (first + i) * 1024);
    }
}
DI void poll_b(const char* ws, int head, int jb, int lane, unsigned token) {
    const int tbk = lane & 15;
    poll_flags(gflag_addr(ws, 1, head, jb, lane), lane < 48 && tbk <= 2 * jb + 1 && tbk >= (lane < 32 ? 2 * jb - 2 : 2 * jb), token);
}

constexpr int G_STAGE = 28672, G_XB = 16384;

#define G_RD_X(NX, XA, XO)        "ds_read_b128 v[" #NX ":" #NX "+3], v" #XA " offset:" XO "\n\t"
#define G_RD_W(NW, WA, WO, C)     "ds_read_b64_tr_b16 v[" #NW ":" #NW "+1], v" #WA " offset:" WO "+" #C "*64\n\t" \
                                  "ds_read_b64_tr_b16 v[" #NW "+2:" #NW "+3], v" #WA " offset:" WO "+768+" #C "*64\n\t"
#define G_MF_T(ACC, X, W)         "v_mfma_f32_32x32x16_bf16 v[" #ACC ":" #ACC "+15], v[" #W ":" #W "+3], v[" #X ":" #X "+3], v[" #ACC ":" #ACC "+15]\n\t"
#define G_MF_C(ACC, X, W)         "v_mfma_f32_32x32x16_bf16 v[" #ACC ":" #ACC "+15], v[" #X ":" #X "+3], v[" #W ":" #W "+3], v[" #ACC ":" #ACC "+15]\n\t"
#define G_STEP(M0, M1, M2, PX, PW0, PW1, PW2, NX, NW0, NW1, NW2, XA, XO, WA, WO, PRE, POST) \
    PRE G_RD_X(NX, XA, XO) G_RD_W(NW0, WA, WO, 0) POST \
    M0(32, PX, PW0) G_RD_W(NW1, WA, WO, 1) M1(48, PX, PW1) G_RD_W(NW2, WA, WO, 2) M2(64, PX, PW2)
#define G_STEP_A(M0, M1, M2, XA, XO, WA, WO, PRE, POST) G_STEP(M0, M1, M2, 80, 84, 88, 92, 96, 100, 104, 108, XA, XO, WA, WO, PRE, POST)
#define G_STEP_B(M0, M1, M2, XA, XO, WA, WO, PRE, POST) G_STEP(M0, M1, M2, 96, 100, 104, 108, 80, 84, 88, 92, XA, XO, WA, WO, PRE, POST)
#define G_W3 "s_waitcnt lgkmcnt(3)\n\t"
#define G_STAGE_ASM(M0, M1, M2, X1, X2, X3, SO, WA, NX0, NSO, NWA) \
    G_STEP_A(M0, M1, M2, X1, SO, WA, SO "+16384+3072", "", G_W3) \
    G_STEP_B(M0, M1, M2, X2, SO, WA, SO "+16384+6144", "", G_W3) \
    G_STEP_A(M0, M1, M2, X3, SO, WA, SO "+16384+9216", "", G_W3) \
    G_STEP_B(M0, M1, M2, NX0, NSO, NWA, NSO "+16384", "s_waitcnt lgkmcnt(0)\n\ts_barrier\n\t", "")
#define G_ROUND_ASM(M0, M1, M2) \
    "s_mov_b32 %[cnt], 2\n\t" \
    "1:\n\t" \
    G_STAGE_ASM(M0, M1, M2, 113, 114, 115, "0", 120, 112, "28672", 120) \
    G_STAGE_ASM(M0, M1, M2, 113, 114, 115, "28672", 120, 116, "0", 121) \
    G_STAGE_ASM(M0, M1, M2, 117, 118, 119, "0", 121, 116, "28672", 121) \
    G_STAGE_ASM(M0, M1, M2, 117, 118, 119, "28672", 121, 112, "0", 120) \
    "s_sub_u32 %[cnt], %[cnt], 1\n\t" \
    "s_cmp_lg_u32 %[cnt], 0\n\t" \
    "s_cbranch_scc1 1b\n\t" \
    "s_waitcnt lgkmcnt(0)\n\t" \
    "s_nop 15\n\t"

struct GFrag { u32x4 x, w0, w1, w2; };
template <int VMASK>
DI void g_round(f32x16& a0, f32x16& a1, f32x16& a2, GFrag& f, u32x4 xa, u32x4 xb, unsigned wa, unsigned wb) {
    u32x4 t0, t1, t2, t3;
    int cnt;
#define G_ROUND_CALL(M0, M1, M2) \
    asm volatile(G_ROUND_ASM(M0, M1, M2) \
        : "+{v[32:47]}"(a0), "+{v[48:63]}"(a1), "+{v[64:79]}"(a2), "+{v[80:83]}"(f.x), "+{v[84:87]}"(f.w0), "+{v[88:91]}"(f.w1), "+{v[92:95]}"(f.w2), \
          "=&{v[96:99]}"(t0), "=&{v[100:103]}"(t1), "=&{v[104:107]}"(t2), "=&{v[108:111]}"(t3), [cnt] "=&s"(cnt) \
        : "{v[112:115]}"(xa), "{v[116:119]}"(xb), "{v120}"(wa), "{v121}"(wb) : "memory", "scc")
    if constexpr (VMASK == 0) G_ROUND_CALL(G_MF_T, G_MF_T, G_MF_T);
    else if constexpr (VMASK == 4) G_ROUND_CALL(G_MF_T, G_MF_T, G_MF_C);
    else G_ROUND_CALL(G_MF_C, G_MF_C, G_MF_C);
}
template <int VMASK>
DI void g_epilogue_piece(const f32x16& acc, int c, int u, char* ws, int br, int tb, int cb, int w, int lane) {
    const int t32 = lane & 31, h = lane >> 5, tile = tb * 4 + w;
    const int n0 = cb * 96 + c * 32, type = n0 >> 9, hd0 = (n0 & 511) >> 4;
    u32x4 v;
    if ((VMASK >> c) & 1) {
        char* dst = ws + WS_V + (size_t)br * TB + (size_t)((hd0 + (t32 >> 4)) * 64 + tile) * 1024 + h * 256 + (t32 & 15) * 16;
#pragma unroll
        for (int i = 0; i < 4; ++i) v[i] = cvtpk(acc[8 * u + 2 * i], acc[8 * u + 2 * i + 1]);
        st16_wt(dst + u * 512, v);
    } else {
        const float sc = type == 0 ? QSCALE : 1.0f;
        char* dst = ws + (type == 0 ? WS_Q : WS_K) + (size_t)br * TB + (size_t)(hd0 * 64 + tile) * 1024 + lane * 16;
#pragma unroll
        for (int i = 0; i < 4; ++i) v[i] = cvtpk(acc[8 * u + 2 * i] * sc, acc[8 * u + 2 * i + 1] * sc);
        st16_wt(dst + (size_t)u * 65536, v);
    }
}
template <int VMASK>
DI void g_epilogue_tile(const f32x16& acc, int c, char* ws, int br, int tb, int cb, int w, int lane) {
    g_epilogue_piece<VMASK>(acc, c, 0, ws, br, tb, cb, w, lane);
    g_epilogue_piece<VMASK>(acc, c, 1, ws, br, tb, cb, w, lane);
}
#define BAR() asm volatile("s_barrier" ::: "memory")
constexpr unsigned L_CTRL = 114688;
constexpr unsigned L_ASIG = 131056;
DI void publish4(LDSP char* ctr, char* flag, unsigned token, int lane) {
    asm volatile("s_waitcnt vmcnt(0)" ::: "memory");
    unsigned old = 0;
    if (lane == 0) old = __hip_atomic_fetch_add((LDSP unsigned*)ctr, 1u, __ATOMIC_RELAXED, __HIP_MEMORY_SCOPE_WORKGROUP);
    if (rfl(old) == 3 && lane < 8) st_flag(flag + lane * 2048, token);
}
DI void lds_signal(LDSP char* word, unsigned v) { *(volatile LDSP unsigned*)word = v; asm volatile("s_waitcnt lgkmcnt(0)" ::: "memory"); }
DI void lds_await(LDSP char* word, unsigned initial) {
#pragma unroll 1
    for (int it = 0; it < SPIN_MAX; ++it) {
        if (rfl(*(volatile LDSP unsigned*)word) != initial) break;
        __builtin_amdgcn_s_sleep(1);
    }
    asm volatile("" ::: "memory");
}
template <int VMASK>
DI u32x4 g_mfma_wave(char* ws, int br, int tb, int cb, int w, int lane, LDSP char* lds, unsigned token, bool fused) {
    const unsigned lds0 = (unsigned)(uintptr_t)lds;
    constexpr int PROFK = 0;
    const int w4 = w & 3, t32 = lane & 31, h = lane >> 5, f = ((t32 >> 1) & 7) ^ h;
    u32x4 xa, xb;
#pragma unroll
    for (int ks = 0; ks < 4; ++ks) { xa[ks] = lds0 + (unsigned)(w4 * 32 + t32) * 128u + ((f ^ (2 * ks)) * 16); xb[ks] = xa[ks] + 2 * G_STAGE; }
    const unsigned wa = lds0 + (unsigned)(8 * h + ((lane & 15) >> 2)) * 192u + (16 * ((lane >> 4) & 1) + 4 * (lane & 3)) * 2, wb = wa + 2 * G_STAGE;
    GFrag fr;
    BAR();
    if (br == 0) {
#pragma unroll
        for (int s = 0; s < 8; ++s) BAR();
    }
    STAMP(1);
    asm volatile("ds_read_b128 %0, %4\n\t"
                 "ds_read_b64_tr_b16 v[84:85], %5 offset:16384\n\tds_read_b64_tr_b16 v[86:87], %5 offset:16384+768\n\t"
                 "ds_read_b64_tr_b16 v[88:89], %5 offset:16384+64\n\tds_read_b64_tr_b16 v[90:91], %5 offset:16384+64+768\n\t"
                 "ds_read_b64_tr_b16 v[92:93], %5 offset:16384+128\n\tds_read_b64_tr_b16 v[94:95], %5 offset:16384+128+768\n\t"
                 "s_waitcnt lgkmcnt(0)"
                 : "=&{v[80:83]}"(fr.x), "=&{v[84:87]}"(fr.w0), "=&{v[88:91]}"(fr.w1), "=&{v[92:95]}"(fr.w2) : "v"(xa[0]), "v"(wa) : "memory");
    f32x16 acc[3] = {f32x16{}, f32x16{}, f32x16{}};
    g_round<VMASK>(acc[0], acc[1], acc[2], fr, xa, xb, wa, wb);
    STAMP(2);
    char* flag = ws + F_G + (br * 128 + cb) * 128 + tb * 4;
    const int bid = blockIdx.x, head = (bid & 7) * 4 + (bid >> 6), jb = (bid >> 3) & 7;
    const char* qp = ws + WS_Q + TB + (size_t)(head * 64 + jb * 8 + w) * 1024;
    u32x4 qb = {0, 0, 0, 0};
    if (br == 1) {
#define PLAN 1
        g_epilogue_tile<VMASK>(acc[0], 0, ws, br, tb, cb, w4, lane); BAR();
        g_epilogue_tile<VMASK>(acc[1], 1, ws, br, tb, cb, w4, lane); BAR();
        g_epilogue_tile<VMASK>(acc[2], 2, ws, br, tb, cb, w4, lane); BAR();
        STAMP(3);
        BAR();
        if (fused) publish4(lds + L_CTRL, flag, token, lane);
        BAR();
        unsigned pv = token;
        const int tbk = lane & 15;
        const bool want = w == 0 && lane < 48 && tbk <= 2 * jb + 1 && tbk >= (lane < 32 ? 2 * jb - 2 : 2 * jb);
        if (fused && want) asm volatile("global_load_dword %0, %1, off sc1" : "+v"(pv) : "v"(gflag_addr(ws, 1, head, jb, lane)) : "memory");
        BAR(); BAR(); BAR();
        if (fused) {
            STAMP(4);
            if (w == 0) {
                asm volatile("s_waitcnt vmcnt(0)" : "+v"(pv) :: "memory");
                if (!__all(pv == token)) poll_b(ws, head, jb, lane, token);
                lds_signal(lds + L_CTRL + 8, 1);
            } else lds_await(lds + L_CTRL + 8, 0);
            STAMP(5);
            qb = ld16_dev(qp, lane * 16);
            stage_window<4>(ws, lds0, head, jb, w4 * 4, lane);
            asm volatile("s_waitcnt vmcnt(0)" : "+v"(qb) :: "memory");
            STAMP(6);
        }
        BAR();
    } else {
#pragma unroll
        for (int c = 0; c < 3; ++c) g_epilogue_tile<VMASK>(acc[c], c, ws, br, tb, cb, w4, lane);
        STAMP(3);
        if (fused) publish4(lds + L_CTRL + 4, flag, token, lane);
        STAMP(5);
        BAR();
    }
    return qb;
}
template <int NX>
DI void g_loader_wave(const char* xA, const char* wA, const char* xB, const char* wB, char* ws, int tb, int cb, int rot, int w, int lane, unsigned lds0) {
    constexpr int PROFK = 0;
    const int j = w & 3;
    unsigned go[NX], ll[NX];
#pragma unroll
    for (int i = 0; i < NX; ++i) {
        const int p = j + 4 * i;
        if (NX == 4) {
            const int row = 8 * p + (lane >> 3), sl = (lane & 7) ^ ((row >> 1) & 7);
            go[i] = (unsigned)(tb * 128 + row) * 1024u + sl * 16; ll[i] = p * 1024;
        } else {
            const int o = p * 1024 + lane * 16, k = o / 192;
            go[i] = (unsigned)k * 3072u + cb * 192 + (o - k * 192); ll[i] = G_XB + p * 1024;
        }
    }
    auto issue = [&](int s) {
        const int ks = (s + rot) & 7;
        const char* src = NX == 4 ? (s < 8 ? xB : xA) + ks * 128 : (s < 8 ? wB : wA) + (size_t)ks * 64 * 3072;
        const unsigned slot = lds0 + (s & 3) * G_STAGE;
#pragma unroll
        for (int i = 0; i < NX; ++i) glds16(src, go[i], slot + ll[i]);
    };
    issue(0); issue(1);
    if (NX == 4) WAITV_BAR(4); else WAITV_BAR(3);
    issue(2); issue(3);
#pragma unroll
    for (int s = 0; s < 16; ++s) {
        if (s <= 12) { if (NX == 4) WAITV_BAR(8); else WAITV_BAR(6); }
        else if (s == 13) { if (NX == 4) WAITV_BAR(4); else WAITV_BAR(3); }
        else WAITV_BAR(0);
        if (w == 15) STAMPX(s + 1);
        if (s + 4 < 16) issue(s + 4);
    }
    BAR();
}
DI u32x4 phase_qkv(const char* xA, const char* wA, const char* xB, const char* wB, char* ws, LDSP char* lds, unsigned token, bool fused) {
    constexpr int PROFK = 0;
    const int lane = threadIdx.x & 63, w = rfl(threadIdx.x >> 6);
    const int bid = blockIdx.x, xcd = bid & 7, idx = bid >> 3;
    const int tb = (xcd & 3) * 4 + (idx & 3), cb = (xcd >> 2) * 8 + (idx >> 2);
    const int rot = (idx >> 2) + 2 * (idx & 3);
    const unsigned lds0 = (unsigned)(uintptr_t)lds;
    STAMP(0);
    if (w == 15) STAMPX(0);
    u32x4 qb = {0, 0, 0, 0};
    if (threadIdx.x == 0) {
        *(LDSP unsigned*)(lds + L_CTRL) = 0; *(LDSP unsigned*)(lds + L_CTRL + 4) = 0; *(LDSP unsigned*)(lds + L_CTRL + 8) = 0;
        *(LDSP unsigned*)(lds + L_ASIG) = ~token;
        asm volatile("s_waitcnt lgkmcnt(0)" ::: "memory");
    }
    if (w < 8) {
        const int br = w < 4 ? 1 : 0;
        if (cb < 10) qb = g_mfma_wave<0>(ws, br, tb, cb, w, lane, lds, token, fused);
        else if (cb == 10) qb = g_mfma_wave<4>(ws, br, tb, cb, w, lane, lds, token, fused);
        else qb = g_mfma_wave<7>(ws, br, tb, cb, w, lane, lds, token, fused);
    } else if (w < 12) g_loader_wave<4>(xA, wA, xB, wB, ws, tb, cb, rot, w, lane, lds0);
    else g_loader_wave<3>(xA, wA, xB, wB, ws, tb, cb, rot, w, lane, lds0);
    STAMP(7);
    return qb;
}

constexpr float SAFE_L = 1.2676506e30f;
constexpr float SAFE_O = 1.3292280e36f;
constexpr float NEG_BIG = -3.0e38f;
constexpr unsigned ONES = 0x3F803F80u;

DI float xhalf_max(float m) {
    auto r = __builtin_amdgcn_permlane32_swap(__float_as_uint(m), __float_as_uint(m), false, false);
    return fmaxf(__uint_as_float(r[0]), __uint_as_float(r[1]));
}
DI float max16(const f32x16& s) {
    float a = fmaxf(fmaxf(s[0], s[1]), s[2]), b = fmaxf(fmaxf(s[3], s[4]), s[5]);
    a = fmaxf(fmaxf(a, s[6]), s[7]); b = fmaxf(fmaxf(b, s[8]), s[9]);
    a = fmaxf(fmaxf(a, s[10]), s[11]); b = fmaxf(fmaxf(b, s[12]), s[13]);
    a = fmaxf(fmaxf(a, s[14]), s[15]);
    return fmaxf(a, b);
}
DI f32x16 bcast16(float v) { f32x16 r; for (int i = 0; i < 16; ++i) r[i] = v; return r; }
DI u32x4 v_init(int lane) { const unsigned x = ((lane & 31) == 16 || (lane & 31) == 20) ? ONES : 0u; return u32x4{x, x, x, x}; }
DI void load_v(u32x4& v0, u32x4& v1, unsigned addr) {
    asm volatile("s_mov_b64 exec, %3\n\tds_read_b128 %0, %2\n\tds_read_b128 %1, %2 offset:512\n\ts_mov_b64 exec, -1\n\ts_waitcnt lgkmcnt(0)"
                 : "+v"(v0), "+v"(v1) : "v"(addr), "s"(0x0000FFFF0000FFFFull) : "memory");
}
DI void pv_regs(const f32x16& p, f32x16& o, const u32x4& v0, const u32x4& v1) {
    u32x4 p0, p1;
#pragma unroll
    for (int i = 0; i < 4; ++i) { p0[i] = cvtpk(p[2 * i], p[2 * i + 1]); p1[i] = cvtpk(p[8 + 2 * i], p[9 + 2 * i]); }
    o = mfma(__builtin_bit_cast(bf16x8, v0), __builtin_bit_cast(bf16x8, p0), o);
    o = mfma(__builtin_bit_cast(bf16x8, v1), __builtin_bit_cast(bf16x8, p1), o);
}
DI void mask_tile(f32x16& s, int mode, int z) {
    if (mode == 0) return;
#pragma unroll
    for (int r = 0; r < 16; ++r) {
        const int cr = (r & 3) + 8 * (r >> 2);
        const bool keep = mode == 1 ? (cr <= z) : (cr > z);
        s[r] = keep ? s[r] : -INFINITY;
    }
}
DI bool unsafe_state(const f32x16& o) {
    float mx = fabsf(o[0]);
#pragma unroll
    for (int i = 1; i < 8; ++i) mx = fmaxf(mx, fabsf(o[i]));
    bool nan = false;
#pragma unroll
    for (int i = 0; i < 9; ++i) nan |= !(o[i] == o[i]);
    return nan || !(mx < SAFE_O) || !(o[8] < SAFE_L);
}
DI void safe_tiles(const char* Kg, const char* Vg, int t0, int n, int step, int first_mode, int last_mode, bf16x8 q, int lane, f32x16& o, float& m) {
    const int z = (lane & 31) - 4 * (lane >> 5);
    const unsigned vl = (lane >> 5) * 256 + (lane & 15) * 16;
    u32x4 v0 = v_init(lane), v1 = v0;
    o = f32x16{}; m = NEG_BIG;
    for (int i = 0; i < n; ++i) {
        const int t = t0 + i * step;
        u32x4 kr = ld16_dev(Kg + (size_t)t * 1024, lane * 16), a0 = ld16_dev(Vg + (size_t)t * 1024, vl), a1 = ld16_dev(Vg + (size_t)t * 1024, vl + 512);
        asm volatile("s_waitcnt vmcnt(0)" : "+v"(kr), "+v"(a0), "+v"(a1) :: "memory");
        const bf16x8 kf = __builtin_bit_cast(bf16x8, kr);
        if ((lane & 16) == 0) { v0 = a0; v1 = a1; }
        f32x16 s = mfma(kf, q, f32x16{});
        mask_tile(s, i == 0 ? first_mode : (i == n - 1 ? last_mode : 0), z);
        const float mn = fmaxf(m, xhalf_max(max16(s)));
        const float al = __builtin_amdgcn_exp2f(m - mn);
        m = mn;
#pragma unroll
        for (int r = 0; r < 16; ++r) { o[r] *= al; s[r] = __builtin_amdgcn_exp2f(s[r] - mn); }
        pv_regs(s, o, v0, v1);
    }
}

#define T_EXP(C, i) "v_exp_f32 v[" #C "+" #i "], v[" #C "+" #i "]\n\t"
#define T_CVT(C, d, a, b) "v_cvt_pk_bf16_f32 v[" #C "+" #d "], v[" #C "+" #a "], v[" #C "+" #b "]\n\t"
#define T_WAIT(n) "s_waitcnt lgkmcnt(" #n ")\n\t"
#define T_QK(N, CR) "v_mfma_f32_32x32x16_bf16 v[" #N ":" #N "+15], v[68:71], v[64:67], v[" #CR ":" #CR "+15]\n\t"
#define T_KRD(KOFF) "ds_read_b128 v[68:71], v30 offset:" #KOFF "\n\t"
#define X_TILE(C, PV1P, QKN, W0, VRD) \
    T_EXP(C, 0) T_EXP(C, 1) T_EXP(C, 2) T_EXP(C, 3) \
    PV1P \
    T_EXP(C, 4) T_EXP(C, 5) T_EXP(C, 6) T_EXP(C, 7) T_EXP(C, 8) \
    QKN \
    T_CVT(C, 0, 0, 1) T_CVT(C, 1, 2, 3) T_CVT(C, 2, 4, 5) T_CVT(C, 3, 6, 7) T_EXP(C, 9) T_EXP(C, 10) \
    W0 \
    "v_mfma_f32_32x32x16_bf16 v[96:111], v[72:75], v[" #C ":" #C "+3], v[96:111]\n\t" \
    VRD \
    T_EXP(C, 11) T_EXP(C, 12) T_EXP(C, 13) T_EXP(C, 14) T_EXP(C, 15) T_CVT(C, 4, 8, 9) T_CVT(C, 5, 10, 11) T_CVT(C, 6, 12, 13) T_CVT(C, 7, 14, 15)
#define T_PV1(P) "v_mfma_f32_32x32x16_bf16 v[96:111], v[76:79], v[" #P "+4:" #P "+7], v[96:111]\n\t"
#define T_XON "s_mov_b64 exec, %[msk]\n\t"
#define T_XOFF "s_mov_b64 exec, -1\n\t"
#define T_V0(O) "ds_read_b128 v[72:75], v31 offset:" #O "\n\t"
#define T_V1(O) "ds_read_b128 v[76:79], v31 offset:" #O "+512\n\t"
#define A_TILE(C, N, KOFF, V1OFF, V0OFF) X_TILE(C, T_WAIT(1) T_PV1(N), T_QK(N, 80) T_KRD(KOFF), T_WAIT(1), T_XON T_V1(V1OFF) T_V0(V0OFF) T_XOFF)
#define A_TILE_FIRST(C, N, KOFF, V1OFF, V0OFF) X_TILE(C, "", T_WAIT(1) T_QK(N, 80) T_KRD(KOFF), T_WAIT(1), T_XON T_V1(V1OFF) T_V0(V0OFF) T_XOFF)
#define T_DRAIN(P) T_WAIT(0) "s_nop 1\n\t" T_PV1(P) "s_nop 15\n\t"

DI void run_b9(unsigned kaddr, unsigned vaddr, bf16x8 q, f32x16 s0, const f32x16& nm, const f32x16& cfar, f32x16& o, u32x4 v0) {
    u32x4 v1 = v0, kf;
    f32x16 s1;
    asm volatile(
        T_KRD(7168) T_XON T_V0(8192) T_XOFF
        A_TILE_FIRST(32, 48, 6144, 8192, 7168)
        A_TILE(48, 32, 5120, 7168, 6144)
        A_TILE(32, 48, 4096, 6144, 5120)
        A_TILE(48, 32, 3072, 5120, 4096)
        A_TILE(32, 48, 2048, 4096, 3072)
        A_TILE(48, 32, 1024, 3072, 2048)
        A_TILE(32, 48, 0, 2048, 1024)
        X_TILE(48, T_WAIT(1) T_PV1(32), T_QK(32, 112), T_WAIT(0), T_XON T_V1(1024) T_V0(0) T_XOFF)
        X_TILE(32, T_WAIT(1) T_PV1(48), "", T_WAIT(0), T_XON T_V1(0) T_XOFF)
        T_DRAIN(32)
        : "+{v[32:47]}"(s0), "=&{v[48:63]}"(s1), "=&{v[68:71]}"(kf), "+{v[72:75]}"(v0), "+{v[76:79]}"(v1), "+{v[96:111]}"(o)
        : "{v30}"(kaddr), "{v31}"(vaddr), "{v[64:67]}"(q), "{v[80:95]}"(nm), "{v[112:127]}"(cfar), [msk] "s"(0x0000FFFF0000FFFFull)
        : "memory");
}

#define A_PAIR(K0) A_TILE(48, 32, K0+2048, K0, K0+1024) A_TILE(32, 48, K0+3072, K0+1024, K0+2048)
#define A_RUN_HEAD \
        "ds_read_b128 v[68:71], v30\n\t" \
        "s_waitcnt lgkmcnt(0)\n\t" \
        "v_mfma_f32_32x32x16_bf16 v[32:47], v[68:71], v[64:67], v[80:95]\n\t" \
        T_KRD(1024) T_XON T_V0(0) T_XOFF \
        "s_nop 7\n\t" \
        "s_nop 3\n\t" \
        A_TILE_FIRST(32, 48, 2048, 0, 1024)
#define A_RUN_TAIL(LAST) X_TILE(48, T_WAIT(1) T_PV1(32), "", T_WAIT(0), T_XON T_V1(LAST) T_XOFF) T_DRAIN(48)
#define A_RUN_OPS \
        : "=&{v[32:47]}"(s0), "=&{v[48:63]}"(s1), "=&{v[68:71]}"(kf), "+{v[72:75]}"(v0), "+{v[76:79]}"(v1), "+{v[96:111]}"(o) \
        : "{v30}"(kaddr), "{v31}"(vaddr), "{v[64:67]}"(q), "{v[80:95]}"(nm), [msk] "s"(0x0000FFFF0000FFFFull) \
        : "memory"
template <int NT>
DI void run_a(unsigned kaddr, unsigned vaddr, bf16x8 q, const f32x16& nm, f32x16& o, u32x4 v0) {
    u32x4 v1 = v0;
    f32x16 s0, s1;
    u32x4 kf;
    if constexpr (NT == 8)
        asm volatile(A_RUN_HEAD A_PAIR(1024) A_PAIR(3072) A_PAIR(5120) A_RUN_TAIL(7168) A_RUN_OPS);
    else
        asm volatile(A_RUN_HEAD A_PAIR(1024) A_PAIR(3072) A_PAIR(5120) A_PAIR(7168) A_PAIR(9216) A_PAIR(11264) A_PAIR(13312) A_PAIR(15360)
                     A_PAIR(17408) A_PAIR(19456) A_PAIR(21504) A_RUN_TAIL(23552) A_RUN_OPS);
}

DI void store_o(char* ws, int br, int qtile, int head, int lane, const f32x16& o) {
    const float inv = 1.0f / o[8];
    u32x4 v;
#pragma unroll
    for (int i = 0; i < 4; ++i) v[i] = cvtpk(o[2 * i] * inv, o[2 * i + 1] * inv);
    st16_wt(ws + WS_O + (size_t)br * TB + (size_t)(qtile * 32 + head) * 1024 + lane * 16, v);
}
DI void put_partial(LDSP float* ex, const f32x16& o, float m) {
#pragma unroll
    for (int r = 0; r < 9; ++r) ex[r * 64] = o[r];
    ex[9 * 64] = m;
}
DI void merge_partial(const LDSP float* ex, f32x16& o, float m) {
    const float m1 = ex[9 * 64], mn = fmaxf(m, m1);
    const float s0 = __builtin_amdgcn_exp2f(m - mn), s1 = __builtin_amdgcn_exp2f(m1 - mn);
#pragma unroll
    for (int r = 0; r < 9; ++r) o[r] = o[r] * s0 + ex[r * 64] * s1;
}
constexpr unsigned L_WOA = 0, L_WOB0 = 131072, L_WOB1 = 98304, L_EX = 65536, L_GCTR = 90112;
DI void wo_issue(const char* Wo, int nb, int p, unsigned dst, int lane, unsigned lds0) {
    const int r = lane >> 3, sl = (lane & 7) ^ (((r >> 1) & 1) << 2);
    glds16(Wo + (size_t)p * 8192, (unsigned)r * 1024u + nb * 128 + sl * 16, lds0 + dst);
}

DI void poll_a(const char* ws, int head, int jb, int lane, unsigned token) {
    poll_flags(gflag_addr(ws, 0, head, jb, lane), lane < 32 || (lane < 48 && ((lane & 15) >> 1) == jb), token);
}

template <bool FUSED>
DI void phase_attn(char* ws, const char* WoA, const char* WoB, int qb, int nb, LDSP char* lds, unsigned token, u32x4 qbr) {
    constexpr int PROFK = 1;
    const unsigned lds0 = (unsigned)(uintptr_t)lds;
    int lane = fresh_lane();
    const int w = rfl(threadIdx.x >> 6);
    const int bid = blockIdx.x, head = (bid & 7) * 4 + (bid >> 6), jb = (bid >> 3) & 7;
    const int qt = w & 7, half = w >> 3, Qt = jb * 8 + qt;
    const char* KA = ws + WS_K + (size_t)head * 65536;
    const char* VA = ws + WS_V + (size_t)head * 65536;
    const char* qp = ws + WS_Q + (size_t)(head * 64 + Qt) * 1024;
    const unsigned lo = lane * 16;
    const int z = (lane & 31) - 4 * (lane >> 5);
    const unsigned vl = (lane >> 5) * 256 + (lane & 15) * 16;
    const u32x4 vinit = v_init(lane);
    STAMP(0);
    if (!FUSED) {
        stage_window<1>(ws, lds0, head, jb, w, lane);
        asm volatile("s_waitcnt vmcnt(0)\n\ts_barrier" ::: "memory");
    }
    u32x4 qar;
    if (half == 0) {
        if (!FUSED || w >= 4) { qbr = ld16_dev(qp + TB, lo); asm volatile("s_waitcnt vmcnt(0)" : "+v"(qbr) :: "memory"); }
        const bf16x8 qbf = __builtin_bit_cast(bf16x8, qbr);
        STAMP(1);
        const int t0 = qt + 8, nt = jb ? 9 : qt + 1;
        f32x16 o = f32x16{};
        f32x16 s = mfma(lds_frag(lds + L_BK + t0 * 1024 + lo), qbf, f32x16{});
        mask_tile(s, 1, z);
        float m = max16(s);
        if (nt > 1) m = fmaxf(m, max16(mfma(lds_frag(lds + L_BK + (t0 - 1) * 1024 + lo), qbf, f32x16{})));
        m = xhalf_max(m);
        const f32x16 nm = bcast16(-m);
        if (jb) {
#pragma unroll
            for (int r = 0; r < 16; ++r) s[r] -= m;
            f32x16 cfar = nm;
            mask_tile(cfar, 2, z);
            run_b9(lds0 + L_BK + qt * 1024 + lo, lds0 + L_BV + qt * 1024 + vl, qbf, s, nm, cfar, o, vinit);
        } else {
            u32x4 v0 = vinit, v1 = vinit;
            for (int i = 0; i < nt; ++i) {
                f32x16 p = mfma(lds_frag(lds + L_BK + (t0 - i) * 1024 + lo), qbf, nm);
                if (i == 0) mask_tile(p, 1, z);
#pragma unroll
                for (int r = 0; r < 16; ++r) p[r] = __builtin_amdgcn_exp2f(p[r]);
                load_v(v0, v1, lds0 + L_BV + (t0 - i) * 1024 + vl);
                pv_regs(p, o, v0, v1);
            }
        }
        lane = fresh_lane();
        if (__any(unsafe_state(o)))
            safe_tiles(ws + WS_K + TB + (size_t)head * 65536, ws + WS_V + TB + (size_t)head * 65536, Qt, nt, -1, 1, jb ? 2 : 0, qbf, lane, o, m);
        store_o(ws, 1, Qt, head, lane, o);
        asm volatile("s_waitcnt vmcnt(0)" ::: "memory");
    } else {
        if (FUSED) {
            if (w == 8) { poll_a(ws, head, jb, lane, token); lds_signal(lds + L_ASIG, token); }
            else lds_await(lds + L_ASIG, ~token);
        }
        STAMP(1);
        qar = ld16_dev(qp, lo);
#pragma unroll
        for (int i = 0; i < 8; ++i) {
            const int t = (i & 1) * 32 + (i >> 1) * 8 + qt;
            glds16_dev(KA + (size_t)t * 1024, lo, lds0 + L_AK + t * 1024);
            glds16_dev(VA + (size_t)t * 1024, lo, lds0 + L_AV + t * 1024);
        }
        asm volatile("s_waitcnt vmcnt(12)" : "+v"(qar) :: "memory");
    }
    STAMP(2);
    asm volatile("s_waitcnt lgkmcnt(0)\n\ts_barrier" ::: "memory");
    STAMP(3);
    if (FUSED && threadIdx.x < 4) st_flag(ws + F_O + ((4 + threadIdx.x) * 8 + jb) * 128 + head * 4, token);
    lane = fresh_lane();
    if (half == 0) { qar = ld16_dev(qp, lane * 16); asm volatile("s_waitcnt vmcnt(0)" : "+v"(qar) :: "memory"); }
    const bf16x8 qa = __builtin_bit_cast(bf16x8, qar);
    f32x16 oa = f32x16{};
    float ma = xhalf_max(max16(mfma(lds_frag(lds + L_AK + half * 32768 + lane * 16), qa, f32x16{})));
    const f32x16 nma = bcast16(-ma);
    run_a<8>(lds0 + L_AK + half * 32768 + lane * 16, lds0 + L_AV + half * 32768 + (lane >> 5) * 256 + (lane & 15) * 16, qa, nma, oa, v_init(lane));
    asm volatile("s_waitcnt vmcnt(0)\n\ts_barrier" ::: "memory");
    lane = fresh_lane();
    if (w >= 12) {
#pragma unroll
        for (int i = 0; i < 8; ++i) wo_issue(WoB, nb, (w - 12) * 8 + i, L_WOB0 + ((w - 12) * 8 + i) * 1024, lane, lds0);
    }
    run_a<24>(lds0 + L_AK + half * 32768 + 8192 + lane * 16, lds0 + L_AV + half * 32768 + 8192 + (lane >> 5) * 256 + (lane & 15) * 16, qa, nma, oa, v_init(lane));
    lane = fresh_lane();
    STAMP(4);
    if (__any(unsafe_state(oa))) safe_tiles(KA, VA, half * 32, 32, 1, 0, 0, qa, lane, oa, ma);
    asm volatile("s_waitcnt lgkmcnt(0)\n\ts_barrier" ::: "memory");
    STAMP(5);
#pragma unroll
    for (int i = 0; i < 6; ++i) {
        const int p = i * 16 + w;
        if (i < 2) wo_issue(WoB, nb, 32 + p, L_WOB1 + p * 1024, lane, lds0);
        else wo_issue(WoA, nb, p - 32, L_WOA + (p - 32) * 1024, lane, lds0);
    }
    if (threadIdx.x == 0) { *(LDSP unsigned*)(lds + L_GCTR) = 0; *(LDSP unsigned*)(lds + L_GCTR + 4) = 0; *(LDSP unsigned*)(lds + L_GCTR + 8) = ~token; }
    LDSP float* ex = (LDSP float*)(lds + L_EX) + qt * 640 + lane;
    if (half == 1) put_partial(ex, oa, ma);
    if (FUSED && w == 0) poll_flags(ws + F_O + ((4 + (nb & 3)) * 8 + (qb >> 2)) * 128 + (lane & 31) * 4, lane < 32, token);
    asm volatile("s_waitcnt lgkmcnt(0)\n\ts_barrier" ::: "memory");
    if (half == 0) { merge_partial(ex, oa, ma); store_o(ws, 0, Qt, head, lane, oa); }
    asm volatile("s_waitcnt vmcnt(0)\n\ts_barrier" ::: "memory");
    if (FUSED && threadIdx.x < 4) st_flag(ws + F_O + (threadIdx.x * 8 + jb) * 128 + head * 4, token);
    STAMP(6);
}

DI void group_sync(LDSP char* ctr, unsigned target, int lane) {
    asm volatile("s_waitcnt lgkmcnt(0)" ::: "memory");
    if (lane == 0) __hip_atomic_fetch_add((LDSP unsigned*)ctr, 1u, __ATOMIC_RELAXED, __HIP_MEMORY_SCOPE_WORKGROUP);
#pragma unroll 1
    for (int it = 0; it < SPIN_MAX; ++it) {
        if ((unsigned)rfl(*(volatile LDSP unsigned*)ctr) >= target) break;
        __builtin_amdgcn_s_sleep(1);
    }
    asm volatile("" ::: "memory");
}
template <bool FUSED>
DI void phase_out(char* ws, char* out, int qb, int nb, LDSP char* lds, unsigned token) {
    constexpr int PROFK = 2;
    const int lane = threadIdx.x & 63, w = rfl(threadIdx.x >> 6);
    const int br = w < 8 ? 1 : 0, qt = w & 1, kq = (w >> 1) & 3;
    STAMP(0);
    if (FUSED && br == 0) {
        if (w == 8) { poll_flags(ws + F_O + ((nb & 3) * 8 + (qb >> 2)) * 128 + (lane & 31) * 4, lane < 32, token); lds_signal(lds + L_GCTR + 8, token); }
        else lds_await(lds + L_GCTR + 8, ~token);
    }
    STAMP(1);
    const char* Of = ws + WS_O + (size_t)br * TB + (size_t)(qb * 2 + qt) * 32768 + kq * 8192;
    u32x4 of[8];
#pragma unroll
    for (int i = 0; i < 8; ++i) of[i] = ld16_dev(Of + i * 1024, lane * 16);
    const int h = lane >> 5, g1 = (lane >> 4) & 1, q = (lane & 15) >> 2, p = lane & 3;
    const unsigned wbase = (br == 0 ? L_WOA + kq * 16384 : (kq < 2 ? L_WOB0 + kq * 16384 : L_WOB1 + (kq - 2) * 16384)) + (unsigned)(4 * h + q) * 128u;
    unsigned wa[2];
#pragma unroll
    for (int nt = 0; nt < 2; ++nt) wa[nt] = wbase + (((nt * 32 + 16 * (p & 1) + 8 * g1 + 4 * (p >> 1)) * 2) ^ (((q >> 1) & 1) << 6));
    f32x16 acc[2] = {f32x16{}, f32x16{}};
    bf16x8 wf[2][2];
#pragma unroll
    for (int nt = 0; nt < 2; ++nt) wf[0][nt] = cat44(lds_tr(lds + wa[nt]), lds_tr(lds + wa[nt] + 1024));
#pragma unroll
    for (int i = 0; i < 8; ++i) {
        if (i < 7) {
#pragma unroll
            for (int nt = 0; nt < 2; ++nt) wf[(i + 1) & 1][nt] = cat44(lds_tr(lds + wa[nt] + (i + 1) * 2048), lds_tr(lds + wa[nt] + (i + 1) * 2048 + 1024));
        }
        asm volatile("s_waitcnt vmcnt(%1)" : "+v"(of[i]) : "i"(7 - i) : "memory");
#pragma unroll
        for (int nt = 0; nt < 2; ++nt) acc[nt] = mfma(wf[i & 1][nt], __builtin_bit_cast(bf16x8, of[i]), acc[nt]);
    }
    STAMP(2);
    LDSP char* ctr = lds + L_GCTR + br * 4;
    group_sync(ctr, 8, lane);
    LDSP f32x4* ex = (LDSP f32x4*)(lds + (br == 0 ? L_WOA : L_WOB1)) + qt * 2048 + lane;
#pragma unroll
    for (int g = 0; g < 4; ++g) {
        const int nt = g >> 1, u = g & 1;
        ex[(g * 4 + kq) * 128] = f32x4{acc[nt][8 * u], acc[nt][8 * u + 1], acc[nt][8 * u + 2], acc[nt][8 * u + 3]};
        ex[(g * 4 + kq) * 128 + 64] = f32x4{acc[nt][8 * u + 4], acc[nt][8 * u + 5], acc[nt][8 * u + 6], acc[nt][8 * u + 7]};
    }
    group_sync(ctr, 16, lane);
    float r[8] = {0, 0, 0, 0, 0, 0, 0, 0};
#pragma unroll
    for (int k = 0; k < 4; ++k) {
        const f32x4 a = ex[(kq * 4 + k) * 128], b = ex[(kq * 4 + k) * 128 + 64];
#pragma unroll
        for (int e = 0; e < 4; ++e) { r[e] += a[e]; r[4 + e] += b[e]; }
    }
    u32x4 v;
#pragma unroll
    for (int i = 0; i < 4; ++i) v[i] = cvtpk(r[2 * i], r[2 * i + 1]);
    char* dst = out + (size_t)((qb * 2 + qt) * 32 + (lane & 31)) * 2048 + br * 1024 + nb * 128 + (kq >> 1) * 64 + h * 32 + (kq & 1) * 16;
    *(u32x4*)dst = v;
    STAMP(3);
}

template <int PH>
__global__ __launch_bounds__(1024) void k_main(const char* xA, const char* wA, const char* oA, const char* xB, const char* wB, const char* oB, char* ws, char* out) {
    extern __shared__ __attribute__((aligned(16))) char smem[];
    LDSP char* lds = (LDSP char*)smem;
    constexpr bool FUSED = PH == 7;
    const int bid = blockIdx.x, xcd = bid & 7, idx = bid >> 3;
    const int qb = xcd * 4 + (idx & 3), nb = idx >> 2;
    unsigned token = 0;
    { constexpr int PROFK = PH == 7 ? 0 : (PH == 1 ? 0 : PH == 2 ? 1 : 2); RSTAMP(0); }
    if (FUSED) token = rfl(ld_flag(ws + F_EPOCH)) + 1;
    u32x4 qfrag = {0, 0, 0, 0};
    if (PH & 1) qfrag = phase_qkv(xA, wA, xB, wB, ws, lds, token, FUSED);
    if (PH & 2) phase_attn<FUSED>(ws, oA, oB, qb, nb, lds, token, qfrag);
    if (PH == 4) {
        const int lane = threadIdx.x & 63, w = rfl(threadIdx.x >> 6);
        const unsigned lds0 = (unsigned)(uintptr_t)lds;
        for (int i = 0; i < 4; ++i) {
            const int p = i * 16 + w;
            wo_issue(oA, nb, p, L_WOA + p * 1024, lane, lds0);
            wo_issue(oB, nb, p, p < 32 ? L_WOB0 + p * 1024 : L_WOB1 + (p - 32) * 1024, lane, lds0);
        }
        if (threadIdx.x == 0) { *(LDSP unsigned*)(lds + L_GCTR) = 0; *(LDSP unsigned*)(lds + L_GCTR + 4) = 0; }
        asm volatile("s_waitcnt vmcnt(0) lgkmcnt(0)\n\ts_barrier" ::: "memory");
    }
    if (PH & 4) phase_out<FUSED>(ws, out, qb, nb, lds, token);
    if (FUSED && bid == 0 && threadIdx.x < 64) {
        const int lane = threadIdx.x;
#pragma unroll
        for (int i = 0; i < 4; ++i) poll_flags(ws + F_G + (i * 4 + (lane >> 4)) * 128 + (lane & 15) * 4, true, token);
        if (lane == 0) st_flag(ws + F_EPOCH, token);
    }
    { constexpr int PROFK = PH == 7 ? 0 : (PH == 1 ? 0 : PH == 2 ? 1 : 2); RSTAMP(1); }
}

extern "C" void kernel_launch(void* const* d_in, const int* in_sizes, int n_in, void* d_out, int out_size, void* d_ws, size_t ws_size,
                              hipStream_t stream) {
    const char* xA = (const char*)d_in[0];
    const char* wA = (const char*)d_in[1];
    const char* oA = (const char*)d_in[2];
    const char* xB = (const char*)d_in[3];
    const char* wB = (const char*)d_in[4];
    const char* oB = (const char*)d_in[5];
    char* ws = (char*)d_ws;
    char* out = (char*)d_out;
    (void)hipFuncSetAttribute((const void*)k_main<7>, hipFuncAttributeMaxDynamicSharedMemorySize, 163840);
    k_main<7><<<256, 1024, 163840, stream>>>(xA, wA, oA, xB, wB, oB, ws, out);
}
```

```cpp
#include <hip/hip_runtime.h>
#include <hip/hip_bf16.h>
#include <stdint.h>
#include <type_traits>

typedef __bf16 bf16x8 __attribute__((ext_vector_type(8)));
typedef __bf16 bf16x2 __attribute__((ext_vector_type(2)));
typedef short s16x4 __attribute__((ext_vector_type(4)));
typedef short s16x8 __attribute__((ext_vector_type(8)));
typedef float f32x16 __attribute__((ext_vector_type(16)));
typedef float f32x4 __attribute__((ext_vector_type(4)));
typedef float f32x2 __attribute__((ext_vector_type(2)));
typedef unsigned u32x4 __attribute__((ext_vector_type(4)));
#define LDSP __attribute__((address_space(3)))
#define DI __device__ __forceinline__

constexpr size_t TB = 2u << 20;
constexpr size_t WS_Q = 0, WS_K = 2 * TB, WS_V = 4 * TB, WS_O = 6 * TB;
constexpr float QSCALE = 0.25f * 1.4426950408889634f;

DI unsigned cvtpk(float lo, float hi) {
    f32x2 v = {lo, hi};
    bf16x2 b = __builtin_convertvector(v, bf16x2);
    return __builtin_bit_cast(unsigned, b);
}
DI f32x16 mfma(bf16x8 a, bf16x8 b, f32x16 c) { return __builtin_amdgcn_mfma_f32_32x32x16_bf16(a, b, c, 0, 0, 0); }
DI int rfl(int x) { return __builtin_amdgcn_readfirstlane(x); }
DI const char* uni(const void* p) {
    const uint64_t a = (uint64_t)p;
    return (const char*)(((uint64_t)(unsigned)rfl((int)(a >> 32)) << 32) | (unsigned)rfl((int)a));
}
DI void glds16(const void* sbase, unsigned voff, unsigned lds_base) {
    unsigned sv;
    asm volatile("s_mov_b32 %0, m0\n\ts_mov_b32 m0, %3\n\ts_nop 2\n\tglobal_load_lds_dwordx4 %1, %2\n\ts_mov_b32 m0, %0"
                 : "=&s"(sv) : "v"(voff), "s"(uni(sbase)), "s"(rfl((int)lds_base)) : "memory");
}
DI void glds16_dev(const void* sbase, unsigned voff, unsigned lds_base) {
    unsigned sv;
    asm volatile("s_mov_b32 %0, m0\n\ts_mov_b32 m0, %3\n\ts_nop 2\n\tglobal_load_lds_dwordx4 %1, %2 sc1\n\ts_mov_b32 m0, %0"
                 : "=&s"(sv) : "v"(voff), "s"(uni(sbase)), "s"(rfl((int)lds_base)) : "memory");
}
DI int fresh_lane() { int l; asm volatile("v_mbcnt_lo_u32_b32 %0, -1, 0\n\tv_mbcnt_hi_u32_b32 %0, -1, %0" : "=v"(l)); return l; }
DI bf16x8 lds_frag(const LDSP char* p) { return *(const LDSP bf16x8*)p; }
DI s16x4 lds_tr(const LDSP char* p) { return __builtin_amdgcn_ds_read_tr16_b64_v4i16((LDSP s16x4*)p); }
DI bf16x8 cat44(s16x4 a, s16x4 b) {
    s16x8 r = {a[0], a[1], a[2], a[3], b[0], b[1], b[2], b[3]};
    return __builtin_bit_cast(bf16x8, r);
}
#define PROF_ARG
#define PROF_PASS
#define SX(i)
#define RSTAMP(i)
#define WAIT_BAR(N) asm volatile("s_waitcnt vmcnt(" #N ") lgkmcnt(0)\n\ts_barrier" ::: "memory")
#define WAITV_BAR(N) asm volatile("s_waitcnt vmcnt(" #N ")\n\ts_barrier" ::: "memory")

constexpr size_t WS_F = 8 * TB;
constexpr size_t F_EPOCH = WS_F, F_G = WS_F + 4096, F_O = F_G + 32768;
constexpr int SPIN_MAX = 1 << 18;
typedef __attribute__((address_space(1))) unsigned gu32;
DI unsigned ld_flag(const char* p) { return __hip_atomic_load((gu32*)p, __ATOMIC_RELAXED, __HIP_MEMORY_SCOPE_AGENT); }
DI void st_flag(char* p, unsigned v) { __hip_atomic_store((gu32*)p, v, __ATOMIC_RELAXED, __HIP_MEMORY_SCOPE_AGENT); }
DI void st16_wt(char* p, u32x4 v) { asm volatile("global_store_dwordx4 %0, %1, off sc1\n\ts_nop 1" :: "v"(p), "v"(v) : "memory"); }
DI void acquire_agent() {
    __builtin_amdgcn_fence(__ATOMIC_ACQUIRE, "agent");
    asm volatile("s_waitcnt vmcnt(0)" ::: "memory");
}
DI u32x4 ld16_dev(const char* sbase, unsigned voff) {
    u32x4 r;
    asm volatile("s_nop 4\n\tglobal_load_dwordx4 %0, %1, %2 sc1" : "=&v"(r) : "v"(voff), "s"(uni(sbase)) : "memory");
    return r;
}
DI void poll_flags(const char* p, unsigned token) {
    unsigned v0, v1, v2, v3;
#define POLL_ISSUE(V) asm volatile("global_load_dword %0, %1, off sc1" : "=v"(V) : "v"(p) : "memory")
#define POLL_STEP(V) asm volatile("s_waitcnt vmcnt(3)" : "+v"(V) :: "memory"); if (__all(V == token)) break; POLL_ISSUE(V); __builtin_amdgcn_s_sleep(2);
    POLL_ISSUE(v0); __builtin_amdgcn_s_sleep(2); POLL_ISSUE(v1); __builtin_amdgcn_s_sleep(2); POLL_ISSUE(v2); __builtin_amdgcn_s_sleep(2); POLL_ISSUE(v3);
#pragma unroll 1
    for (int it = 0; it < SPIN_MAX; ++it) {
        POLL_STEP(v0) POLL_STEP(v1) POLL_STEP(v2) POLL_STEP(v3)
    }
    asm volatile("s_waitcnt vmcnt(0)" : "+v"(v0), "+v"(v1), "+v"(v2), "+v"(v3) :: "memory");
#undef POLL_ISSUE
#undef POLL_STEP
}
DI const char* gflag_addr(const char* ws, int br, int head, int jb, int type, int tb) {
    return ws + F_G + ((br * 8 + jb) * 16 + (type * 512 + head * 16) / 96) * 128 + tb * 4;
}
DI const char* a_flag(const char* ws, int head, int jb, int lane) {
    const int type = lane < 16 ? 1 : (lane < 32 ? 2 : 0);
    return gflag_addr(ws, 0, head, jb, type, lane < 32 ? (lane & 15) : 2 * jb + (lane & 1));
}
DI const char* b_flag(const char* ws, int head, int jb, int lane) {
    const int type = lane < 16 ? 1 : (lane < 32 ? 2 : 0);
    int tb = lane < 32 ? 2 * jb - 2 + (lane & 3) : 2 * jb + (lane & 1);
    if (tb < 0) tb = 0;
    return gflag_addr(ws, 1, head, jb, type, tb);
}

constexpr unsigned L_LEAD = 114688, L_BK = 114688, L_BV = 131072, L_C = 147456, L_QA = 153600;
constexpr unsigned L_T = L_C + 64;
DI void lds_st16(LDSP char* p, u32x4 v) { *(LDSP u32x4*)p = v; }
DI void stage_lead(const char* ws, unsigned lds0, int head, int jb, int w, int lane) {
    const char* KA = ws + WS_K + (size_t)head * 65536;
    const char* VA = ws + WS_V + (size_t)head * 65536;
#pragma unroll
    for (int i = 0; i < 4; ++i) {
        const int e = w * 4 + i, h = e >> 3, t = e & 7;
        glds16_dev(KA + (size_t)(h * 32 + t) * 1024, lane * 16, lds0 + L_LEAD + h * 16384 + t * 1024);
        glds16_dev(VA + (size_t)(h * 32 + t) * 1024, lane * 16, lds0 + L_LEAD + h * 16384 + 8192 + t * 1024);
    }
#pragma unroll
    for (int i = 0; i < 2; ++i) glds16_dev(ws + WS_Q + (size_t)(head * 64 + jb * 8 + w * 2 + i) * 1024, lane * 16, lds0 + L_QA + (w * 2 + i) * 1024);
}
template <int N>
DI void stage_window(const char* ws, unsigned lds0, int head, int jb, int first, int lane) {
    const char* KB = ws + WS_K + TB + (size_t)head * 65536;
    const char* VB = ws + WS_V + TB + (size_t)head * 65536;
#pragma unroll
    for (int i = 0; i < N; ++i) {
        int t = jb * 8 - 8 + first + i; if (t < 0) t = 0;
        glds16_dev(KB + (size_t)t * 1024, lane * 16, lds0 + L_BK + (first + i) * 1024);
        glds16_dev(VB + (size_t)t * 1024, lane * 16, lds0 + L_BV + (first + i) * 1024);
    }
}

constexpr int G_STAGE = 28672, G_XB = 16384;

#define G_RD_X(NX, XA, XO)        "ds_read_b128 v[" #NX ":" #NX "+3], v" #XA " offset:" XO "\n\t"
#define G_RD_W(NW, WA, WO, C)     "ds_read_b64_tr_b16 v[" #NW ":" #NW "+1], v" #WA " offset:" WO "+" #C "*64\n\t" \
                                  "ds_read_b64_tr_b16 v[" #NW "+2:" #NW "+3], v" #WA " offset:" WO "+768+" #C "*64\n\t"
#define G_MF_T(ACC, X, W)         "v_mfma_f32_32x32x16_bf16 v[" #ACC ":" #ACC "+15], v[" #W ":" #W "+3], v[" #X ":" #X "+3], v[" #ACC ":" #ACC "+15]\n\t"
#define G_MF_C(ACC, X, W)         "v_mfma_f32_32x32x16_bf16 v[" #ACC ":" #ACC "+15], v[" #X ":" #X "+3], v[" #W ":" #W "+3], v[" #ACC ":" #ACC "+15]\n\t"
#define G_STEP(M0, M1, M2, PX, PW0, PW1, PW2, NX, NW0, NW1, NW2, XA, XO, WA, WO, PRE, POST) \
    PRE G_RD_X(NX, XA, XO) G_RD_W(NW0, WA, WO, 0) POST \
    M0(32, PX, PW0) G_RD_W(NW1, WA, WO, 1) M1(48, PX, PW1) G_RD_W(NW2, WA, WO, 2) M2(64, PX, PW2)
#define G_STEP_A(M0, M1, M2, XA, XO, WA, WO, PRE, POST) G_STEP(M0, M1, M2, 80, 84, 88, 92, 96, 100, 104, 108, XA, XO, WA, WO, PRE, POST)
#define G_STEP_B(M0, M1, M2, XA, XO, WA, WO, PRE, POST) G_STEP(M0, M1, M2, 96, 100, 104, 108, 80, 84, 88, 92, XA, XO, WA, WO, PRE, POST)
#define G_W3 "s_waitcnt lgkmcnt(3)\n\t"
#define G_STAGE_ASM(M0, M1, M2, X1, X2, X3, SO, WA, NX0, NSO, NWA) \
    G_STEP_A(M0, M1, M2, X1, SO, WA, SO "+16384+3072", "", G_W3) \
    G_STEP_B(M0, M1, M2, X2, SO, WA, SO "+16384+6144", "", G_W3) \
    G_STEP_A(M0, M1, M2, X3, SO, WA, SO "+16384+9216", "", G_W3) \
    G_STEP_B(M0, M1, M2, NX0, NSO, NWA, NSO "+16384", "s_waitcnt lgkmcnt(0)\n\ts_barrier\n\t", "")
#define G_ROUND_ASM(M0, M1, M2) \
    "s_mov_b32 %[cnt], 2\n\t" \
    "1:\n\t" \
    G_STAGE_ASM(M0, M1, M2, 113, 114, 115, "0", 120, 112, "28672", 120) \
    G_STAGE_ASM(M0, M1, M2, 113, 114, 115, "28672", 120, 116, "0", 121) \
    G_STAGE_ASM(M0, M1, M2, 117, 118, 119, "0", 121, 116, "28672", 121) \
    G_STAGE_ASM(M0, M1, M2, 117, 118, 119, "28672", 121, 112, "0", 120) \
    "s_sub_u32 %[cnt], %[cnt], 1\n\t" \
    "s_cmp_lg_u32 %[cnt], 0\n\t" \
    "s_cbranch_scc1 1b\n\t" \
    "s_waitcnt lgkmcnt(0)\n\t" \
    "s_nop 15\n\t"

struct GFrag { u32x4 x, w0, w1, w2; };
template <int VMASK>
DI void g_round(f32x16& a0, f32x16& a1, f32x16& a2, GFrag& f, u32x4 xa, u32x4 xb, unsigned wa, unsigned wb) {
    u32x4 t0, t1, t2, t3;
    int cnt;
#define G_ROUND_CALL(M0, M1, M2) \
    asm volatile(G_ROUND_ASM(M0, M1, M2) \
        : "+{v[32:47]}"(a0), "+{v[48:63]}"(a1), "+{v[64:79]}"(a2), "+{v[80:83]}"(f.x), "+{v[84:87]}"(f.w0), "+{v[88:91]}"(f.w1), "+{v[92:95]}"(f.w2), \
          "=&{v[96:99]}"(t0), "=&{v[100:103]}"(t1), "=&{v[104:107]}"(t2), "=&{v[108:111]}"(t3), [cnt] "=&s"(cnt) \
        : "{v[112:115]}"(xa), "{v[116:119]}"(xb), "{v120}"(wa), "{v121}"(wb) : "memory", "scc")
    if constexpr (VMASK == 0) G_ROUND_CALL(G_MF_T, G_MF_T, G_MF_T);
    else if constexpr (VMASK == 4) G_ROUND_CALL(G_MF_T, G_MF_T, G_MF_C);
    else G_ROUND_CALL(G_MF_C, G_MF_C, G_MF_C);
}
template <int VMASK>
DI void g_epilogue_piece(const f32x16& acc, int c, int u, char* ws, int br, int tb, int cb, int w, int lane) {
    const int t32 = lane & 31, h = lane >> 5, tile = tb * 4 + w;
    const int n0 = cb * 96 + c * 32, type = n0 >> 9, hd0 = (n0 & 511) >> 4;
    u32x4 v;
    if ((VMASK >> c) & 1) {
        char* dst = ws + WS_V + (size_t)br * TB + (size_t)((hd0 + (t32 >> 4)) * 64 + tile) * 1024 + h * 256 + (t32 & 15) * 16;
#pragma unroll
        for (int i = 0; i < 4; ++i) v[i] = cvtpk(acc[8 * u + 2 * i], acc[8 * u + 2 * i + 1]);
        st16_wt(dst + u * 512, v);
    } else {
        const float sc = type == 0 ? QSCALE : 1.0f;
        char* dst = ws + (type == 0 ? WS_Q : WS_K) + (size_t)br * TB + (size_t)(hd0 * 64 + tile) * 1024 + lane * 16;
#pragma unroll
        for (int i = 0; i < 4; ++i) v[i] = cvtpk(acc[8 * u + 2 * i] * sc, acc[8 * u + 2 * i + 1] * sc);
        st16_wt(dst + (size_t)u * 65536, v);
    }
}
template <int VMASK>
DI void g_epilogue_tile(const f32x16& acc, int c, char* ws, int br, int tb, int cb, int w, int lane) {
    g_epilogue_piece<VMASK>(acc, c, 0, ws, br, tb, cb, w, lane);
    g_epilogue_piece<VMASK>(acc, c, 1, ws, br, tb, cb, w, lane);
}
#define BAR() asm volatile("s_barrier" ::: "memory")
DI void publish(LDSP char* ctr, unsigned last, char* flag, int nrep, int rep_stride, unsigned token, int lane) {
    asm volatile("s_waitcnt vmcnt(0)" ::: "memory");
    unsigned old = 0;
    if (lane == 0) old = __hip_atomic_fetch_add((LDSP unsigned*)ctr, 1u, __ATOMIC_RELAXED, __HIP_MEMORY_SCOPE_WORKGROUP);
    if ((unsigned)rfl(old) == last && lane < nrep) st_flag(flag + lane * rep_stride, token);
}
DI void lds_signal(LDSP char* word, unsigned v) { *(volatile LDSP unsigned*)word = v; asm volatile("s_waitcnt lgkmcnt(0)" ::: "memory"); }
template <int SLEEP = 1>
DI void lds_await(LDSP char* word, unsigned initial) {
    if ((unsigned)rfl(*(volatile LDSP unsigned*)word) == initial) {
#pragma unroll 1
        for (int it = 0; it < SPIN_MAX; ++it) {
            __builtin_amdgcn_s_sleep(SLEEP);
            if ((unsigned)rfl(*(volatile LDSP unsigned*)word) != initial) break;
        }
    }
    asm volatile("" ::: "memory");
}
template <int VMASK>
DI void g_mfma_wave(const char* oA, const char* oB, char* ws, int round, int tb, int cb, int w, int lane, LDSP char* lds, unsigned token PROF_ARG) {
    const unsigned lds0 = (unsigned)(uintptr_t)lds;
    const int br = round;
    const int w4 = w & 3, t32 = lane & 31, h = lane >> 5, f = ((t32 >> 1) & 7) ^ h;
    u32x4 xa, xb;
#pragma unroll
    for (int ks = 0; ks < 4; ++ks) { xa[ks] = lds0 + (unsigned)(w4 * 32 + t32) * 128u + ((f ^ (2 * ks)) * 16); xb[ks] = xa[ks] + 2 * G_STAGE; }
    const unsigned wa = lds0 + (unsigned)(8 * h + ((lane & 15) >> 2)) * 192u + (16 * ((lane >> 4) & 1) + 4 * (lane & 3)) * 2, wb = wa + 2 * G_STAGE;
    GFrag fr;
    BAR();
    if (round == 1) {
        u32x4 dummy = {0, 0, 0, 0};
#pragma unroll
        for (int s = 0; s < 9; ++s) {
            BAR();
        }
        asm volatile("s_waitcnt vmcnt(0)" : "+v"(dummy) :: "memory");
    }
    SX(1);
    asm volatile("ds_read_b128 %0, %4\n\t"
                 "ds_read_b64_tr_b16 v[84:85], %5 offset:16384\n\tds_read_b64_tr_b16 v[86:87], %5 offset:16384+768\n\t"
                 "ds_read_b64_tr_b16 v[88:89], %5 offset:16384+64\n\tds_read_b64_tr_b16 v[90:91], %5 offset:16384+64+768\n\t"
                 "ds_read_b64_tr_b16 v[92:93], %5 offset:16384+128\n\tds_read_b64_tr_b16 v[94:95], %5 offset:16384+128+768\n\t"
                 "s_waitcnt lgkmcnt(0)"
                 : "=&{v[80:83]}"(fr.x), "=&{v[84:87]}"(fr.w0), "=&{v[88:91]}"(fr.w1), "=&{v[92:95]}"(fr.w2) : "v"(xa[0]), "v"(wa) : "memory");
    f32x16 acc[3] = {f32x16{}, f32x16{}, f32x16{}};
    g_round<VMASK>(acc[0], acc[1], acc[2], fr, xa, xb, wa, wb);
    SX(2);
    char* flag = ws + F_G + (br * 128 + cb) * 128 + tb * 4;
    if (round == 0) {
#pragma unroll
        for (int c = 0; c < 3; ++c) g_epilogue_tile<VMASK>(acc[c], c, ws, br, tb, cb, w4, lane);
        SX(3);
        publish(lds + L_C, 3, flag, 8, 2048, token, lane);
        SX(4);
        BAR();
        BAR(); BAR(); BAR(); BAR();
        const int bid = blockIdx.x, head = (bid & 7) * 4 + (bid >> 6), jb = (bid >> 3) & 7;
        const char* fa = a_flag(ws, head, jb, lane);
        unsigned pv1, pv2;
        asm volatile("global_load_dword %0, %1, off sc1" : "=v"(pv1) : "v"(fa) : "memory");
        BAR(); BAR();
        asm volatile("s_waitcnt vmcnt(0)" : "+v"(pv1) :: "memory");
        bool staged = __all(pv1 == token);
        asm volatile("global_load_dword %0, %1, off sc1" : "=v"(pv2) : "v"(fa) : "memory");
        if (staged) stage_lead(ws, lds0, head, jb, w, lane);
        BAR(); BAR();
        asm volatile("s_waitcnt vmcnt(10)" : "+v"(pv2) :: "memory");
        if (!staged) {
            asm volatile("s_waitcnt vmcnt(0)" : "+v"(pv2) :: "memory");
            if (__all(pv2 == token)) { stage_lead(ws, lds0, head, jb, w, lane); staged = true; }
        }
        BAR();
        if (!staged) { poll_flags(fa, token); stage_lead(ws, lds0, head, jb, w, lane); }
        if (w == 0) lds_signal(lds + L_C + 8, token);
        asm volatile("s_waitcnt vmcnt(0)" ::: "memory");
        if (lane == 0) __hip_atomic_fetch_add((LDSP unsigned*)(lds + L_C + 12), 1u, __ATOMIC_RELAXED, __HIP_MEMORY_SCOPE_WORKGROUP);
    } else {
        BAR();
#pragma unroll
        for (int c = 0; c < 3; ++c) g_epilogue_tile<VMASK>(acc[c], c, ws, br, tb, cb, w4, lane);
        SX(3);
    }
}
template <int NX>
DI void g_loader_wave(const char* xA, const char* wA, const char* xB, const char* wB, char* ws, int tb, int cb, int rot, int w, int lane, unsigned lds0 PROF_ARG) {
    const int j = w & 3;
    unsigned go[NX], ll[NX];
#pragma unroll
    for (int i = 0; i < NX; ++i) {
        const int p = j + 4 * i;
        if (NX == 4) {
            const int row = 8 * p + (lane >> 3), sl = (lane & 7) ^ ((row >> 1) & 7);
            go[i] = (unsigned)(tb * 128 + row) * 1024u + sl * 16; ll[i] = p * 1024;
        } else {
            const int o = p * 1024 + lane * 16, k = o / 192;
            go[i] = (unsigned)k * 3072u + cb * 192 + (o - k * 192); ll[i] = G_XB + p * 1024;
        }
    }
    auto issue = [&](int s) {
        const int ks = (s + rot) & 7;
        const char* src = NX == 4 ? (s < 8 ? xA : xB) + ks * 128 : (s < 8 ? wA : wB) + (size_t)ks * 64 * 3072;
        const unsigned slot = lds0 + (s & 3) * G_STAGE;
#pragma unroll
        for (int i = 0; i < NX; ++i) glds16(src, go[i], slot + ll[i]);
    };
    issue(0); issue(1);
    if (NX == 4) WAITV_BAR(4); else WAITV_BAR(3);
    issue(2); issue(3);
#define E8 3
    constexpr int early5 = E8 == 3 ? 1 : 0, early6 = E8 == 1 ? 1 : (E8 == 2 ? 2 : (E8 == 3 ? 1 : 0));
#pragma unroll
    for (int s = 0; s < 8; ++s) {
        const int allow = s <= 4 ? 2 : (s == 5 ? 1 : (s == 6 ? early5 : early5 + early6));
        if (allow == 2) { if (NX == 4) WAITV_BAR(8); else WAITV_BAR(6); }
        else if (allow == 1) { if (NX == 4) WAITV_BAR(4); else WAITV_BAR(3); }
        else WAITV_BAR(0);
        if (s == 0) SX(1);
        if (s == 7) SX(2);
        if (s + 4 < 8) issue(s + 4);
        if (s == 5 && early5) issue(8);
        if (s == 6) { if (early6 >= 1) issue(8 + early5); if (early6 >= 2) issue(9 + early5); }
    }
    constexpr int done = 8 + early5 + early6;
    if (done <= 8) issue(8);
    if (done <= 9) issue(9);
    if (NX == 4) WAITV_BAR(4); else WAITV_BAR(3);
    issue(10); issue(11);
#pragma unroll
    for (int s = 8; s < 16; ++s) {
        if (s <= 12) { if (NX == 4) WAITV_BAR(8); else WAITV_BAR(6); }
        else if (s == 13) { if (NX == 4) WAITV_BAR(4); else WAITV_BAR(3); }
        else WAITV_BAR(0);
        if (s == 11) SX(3);
        if (s == 15) SX(4);
        if (s + 4 < 16) issue(s + 4);
    }
    BAR();
}
DI void phase_qkv(const char* xA, const char* wA, const char* oA, const char* xB, const char* wB, const char* oB, char* ws, LDSP char* lds, unsigned token PROF_ARG) {
    const int lane = threadIdx.x & 63, w = rfl(threadIdx.x >> 6);
    const int bid = blockIdx.x, xcd = bid & 7, idx = bid >> 3;
    const int tb = (xcd & 3) * 4 + (idx & 3), cb = (xcd >> 2) * 8 + (idx >> 2);
    const int rot = (idx >> 2) + 2 * (idx & 3);
    const unsigned lds0 = (unsigned)(uintptr_t)lds;
    SX(0);
    if (threadIdx.x == 0) {
#pragma unroll
        for (int i = 0; i < 36; ++i) *(LDSP unsigned*)(lds + L_C + 4 * i) = 0;
        *(LDSP unsigned*)(lds + L_C + 8) = ~token; *(LDSP unsigned*)(lds + L_T + 48) = ~token; *(LDSP unsigned*)(lds + L_T + 52) = ~token;
        asm volatile("s_waitcnt lgkmcnt(0)" ::: "memory");
    }
    if (w < 8) {
        if (cb < 10) g_mfma_wave<0>(oA, oB, ws, w >> 2, tb, cb, w, lane, lds, token PROF_PASS);
        else if (cb == 10) g_mfma_wave<4>(oA, oB, ws, w >> 2, tb, cb, w, lane, lds, token PROF_PASS);
        else g_mfma_wave<7>(oA, oB, ws, w >> 2, tb, cb, w, lane, lds, token PROF_PASS);
    } else if (w < 12) g_loader_wave<4>(xA, wA, xB, wB, ws, tb, cb, rot, w, lane, lds0 PROF_PASS);
    else g_loader_wave<3>(xA, wA, xB, wB, ws, tb, cb, rot, w, lane, lds0 PROF_PASS);
    SX(5);
}

constexpr float SAFE_L = 1.2676506e30f;
constexpr float SAFE_O = 1.3292280e36f;
constexpr float NEG_BIG = -3.0e38f;
constexpr unsigned ONES = 0x3F803F80u;

DI float xhalf_max(float m) {
    auto r = __builtin_amdgcn_permlane32_swap(__float_as_uint(m), __float_as_uint(m), false, false);
    return fmaxf(__uint_as_float(r[0]), __uint_as_float(r[1]));
}
DI float max16(const f32x16& s) {
    float a = fmaxf(fmaxf(s[0], s[1]), s[2]), b = fmaxf(fmaxf(s[3], s[4]), s[5]);
    a = fmaxf(fmaxf(a, s[6]), s[7]); b = fmaxf(fmaxf(b, s[8]), s[9]);
    a = fmaxf(fmaxf(a, s[10]), s[11]); b = fmaxf(fmaxf(b, s[12]), s[13]);
    a = fmaxf(fmaxf(a, s[14]), s[15]);
    return fmaxf(a, b);
}
DI f32x16 bcast16(float v) { f32x16 r; for (int i = 0; i < 16; ++i) r[i] = v; return r; }
DI u32x4 v_init(int lane) { const unsigned x = ((lane & 31) == 16 || (lane & 31) == 20) ? ONES : 0u; return u32x4{x, x, x, x}; }
DI void load_v(u32x4& v0, u32x4& v1, unsigned addr) {
    asm volatile("s_mov_b64 exec, %3\n\tds_read_b128 %0, %2\n\tds_read_b128 %1, %2 offset:512\n\ts_mov_b64 exec, -1\n\ts_waitcnt lgkmcnt(0)"
                 : "+v"(v0), "+v"(v1) : "v"(addr), "s"(0x0000FFFF0000FFFFull) : "memory");
}
DI void pv_regs(const f32x16& p, f32x16& o, const u32x4& v0, const u32x4& v1) {
    u32x4 p0, p1;
#pragma unroll
    for (int i = 0; i < 4; ++i) { p0[i] = cvtpk(p[2 * i], p[2 * i + 1]); p1[i] = cvtpk(p[8 + 2 * i], p[9 + 2 * i]); }
    o = mfma(__builtin_bit_cast(bf16x8, v0), __builtin_bit_cast(bf16x8, p0), o);
    o = mfma(__builtin_bit_cast(bf16x8, v1), __builtin_bit_cast(bf16x8, p1), o);
}
DI void mask_tile(f32x16& s, int mode, int z) {
    if (mode == 0) return;
#pragma unroll
    for (int r = 0; r < 16; ++r) {
        const int cr = (r & 3) + 8 * (r >> 2);
        const bool keep = mode == 1 ? (cr <= z) : (cr > z);
        s[r] = keep ? s[r] : -INFINITY;
    }
}
DI bool unsafe_state(const f32x16& o) {
    float mx = fabsf(o[0]);
#pragma unroll
    for (int i = 1; i < 8; ++i) mx = fmaxf(mx, fabsf(o[i]));
    bool nan = false;
#pragma unroll
    for (int i = 0; i < 9; ++i) nan |= !(o[i] == o[i]);
    return nan || !(mx < SAFE_O) || !(o[8] < SAFE_L);
}
DI void safe_tiles(const char* Kg, const char* Vg, int t0, int n, int step, int first_mode, int last_mode, bf16x8 q, int lane, f32x16& o, float& m) {
    const int z = (lane & 31) - 4 * (lane >> 5);
    const unsigned vl = (lane >> 5) * 256 + (lane & 15) * 16;
    u32x4 v0 = v_init(lane), v1 = v0;
    o = f32x16{}; m = NEG_BIG;
    for (int i = 0; i < n; ++i) {
        const int t = t0 + i * step;
        u32x4 kr = ld16_dev(Kg + (size_t)t * 1024, lane * 16), a0 = ld16_dev(Vg + (size_t)t * 1024, vl), a1 = ld16_dev(Vg + (size_t)t * 1024, vl + 512);
        asm volatile("s_waitcnt vmcnt(0)" : "+v"(kr), "+v"(a0), "+v"(a1) :: "memory");
        const bf16x8 kf = __builtin_bit_cast(bf16x8, kr);
        if ((lane & 16) == 0) { v0 = a0; v1 = a1; }
        f32x16 s = mfma(kf, q, f32x16{});
        mask_tile(s, i == 0 ? first_mode : (i == n - 1 ? last_mode : 0), z);
        const float mn = fmaxf(m, xhalf_max(max16(s)));
        const float al = __builtin_amdgcn_exp2f(m - mn);
        m = mn;
#pragma unroll
        for (int r = 0; r < 16; ++r) { o[r] *= al; s[r] = __builtin_amdgcn_exp2f(s[r] - mn); }
        pv_regs(s, o, v0, v1);
    }
}

#define T_EXP(C, i) "v_exp_f32 v[" #C "+" #i "], v[" #C "+" #i "]\n\t"
#define T_CVT(C, d, a, b) "v_cvt_pk_bf16_f32 v[" #C "+" #d "], v[" #C "+" #a "], v[" #C "+" #b "]\n\t"
#define T_WAIT(n) "s_waitcnt lgkmcnt(" #n ")\n\t"
#define T_QK(N, CR) "v_mfma_f32_32x32x16_bf16 v[" #N ":" #N "+15], v[68:71], v[64:67], v[" #CR ":" #CR "+15]\n\t"
#define T_KRD(KOFF) "ds_read_b128 v[68:71], v30 offset:" #KOFF "\n\t"
#define X_TILE(C, PV1P, QKN, W0, VRD) \
    T_EXP(C, 0) T_EXP(C, 1) T_EXP(C, 2) T_EXP(C, 3) \
    PV1P \
    T_EXP(C, 4) T_EXP(C, 5) T_EXP(C, 6) T_EXP(C, 7) T_EXP(C, 8) \
    QKN \
    T_CVT(C, 0, 0, 1) T_CVT(C, 1, 2, 3) T_CVT(C, 2, 4, 5) T_CVT(C, 3, 6, 7) T_EXP(C, 9) T_EXP(C, 10) \
    W0 \
    "v_mfma_f32_32x32x16_bf16 v[96:111], v[72:75], v[" #C ":" #C "+3], v[96:111]\n\t" \
    VRD \
    T_EXP(C, 11) T_EXP(C, 12) T_EXP(C, 13) T_EXP(C, 14) T_EXP(C, 15) T_CVT(C, 4, 8, 9) T_CVT(C, 5, 10, 11) T_CVT(C, 6, 12, 13) T_CVT(C, 7, 14, 15)
#define T_PV1(P) "v_mfma_f32_32x32x16_bf16 v[96:111], v[76:79], v[" #P "+4:" #P "+7], v[96:111]\n\t"
#define T_XON "s_mov_b64 exec, %[msk]\n\t"
#define T_XOFF "s_mov_b64 exec, -1\n\t"
#define T_V0(O) "ds_read_b128 v[72:75], v31 offset:" #O "\n\t"
#define T_V1(O) "ds_read_b128 v[76:79], v31 offset:" #O "+512\n\t"
#define A_TILE(C, N, KOFF, V1OFF, V0OFF) X_TILE(C, T_WAIT(1) T_PV1(N), T_QK(N, 80) T_KRD(KOFF), T_WAIT(1), T_XON T_V1(V1OFF) T_V0(V0OFF) T_XOFF)
#define A_TILE_FIRST(C, N, KOFF, V1OFF, V0OFF) X_TILE(C, "", T_WAIT(1) T_QK(N, 80) T_KRD(KOFF), T_WAIT(1), T_XON T_V1(V1OFF) T_V0(V0OFF) T_XOFF)
#define T_DRAIN(P, X) T_WAIT(0) "s_nop 1\n\t" T_PV1(P) X "s_nop 15\n\t"

DI void run_b9(unsigned kaddr, unsigned vaddr, bf16x8 q, f32x16 s0, const f32x16& nm, const f32x16& cfar, f32x16& o, u32x4 v0) {
    u32x4 v1 = v0, kf;
    f32x16 s1;
    asm volatile(
        T_KRD(7168) T_XON T_V0(8192) T_XOFF
        A_TILE_FIRST(32, 48, 6144, 8192, 7168)
        A_TILE(48, 32, 5120, 7168, 6144)
        A_TILE(32, 48, 4096, 6144, 5120)
        A_TILE(48, 32, 3072, 5120, 4096)
        A_TILE(32, 48, 2048, 4096, 3072)
        A_TILE(48, 32, 1024, 3072, 2048)
        A_TILE(32, 48, 0, 2048, 1024)
        X_TILE(48, T_WAIT(1) T_PV1(32), T_QK(32, 112), T_WAIT(0), T_XON T_V1(1024) T_V0(0) T_XOFF)
        X_TILE(32, T_WAIT(1) T_PV1(48), "", T_WAIT(0), T_XON T_V1(0) T_XOFF)
        T_DRAIN(32, "")
        : "+{v[32:47]}"(s0), "=&{v[48:63]}"(s1), "=&{v[68:71]}"(kf), "+{v[72:75]}"(v0), "+{v[76:79]}"(v1), "+{v[96:111]}"(o)
        : "{v30}"(kaddr), "{v31}"(vaddr), "{v[64:67]}"(q), "{v[80:95]}"(nm), "{v[112:127]}"(cfar), [msk] "s"(0x0000FFFF0000FFFFull)
        : "memory");
}

#define A_PAIR(K0) A_TILE(48, 32, K0+2048, K0, K0+1024) A_TILE(32, 48, K0+3072, K0+1024, K0+2048)
#define A_RUN_HEAD \
        "s_setprio 1\n\t" \
        "ds_read_b128 v[68:71], v30\n\t" \
        "s_waitcnt lgkmcnt(0)\n\t" \
        "v_mfma_f32_32x32x16_bf16 v[32:47], v[68:71], v[64:67], v[80:95]\n\t" \
        T_KRD(1024) T_XON T_V0(0) T_XOFF \
        "s_nop 7\n\t" \
        "s_nop 3\n\t" \
        A_TILE_FIRST(32, 48, 2048, 0, 1024)
#define A_RUN_TAIL(LAST) X_TILE(48, T_WAIT(1) T_PV1(32), "", T_WAIT(0), T_XON T_V1(LAST) T_XOFF) T_DRAIN(48, "")
#define A_RUN_OPS \
        : "=&{v[32:47]}"(s0), "=&{v[48:63]}"(s1), "=&{v[68:71]}"(kf), "+{v[72:75]}"(v0), "+{v[76:79]}"(v1), "+{v[96:111]}"(o) \
        : "{v30}"(kaddr), "{v31}"(vaddr), "{v[64:67]}"(q), "{v[80:95]}"(nm), [msk] "s"(0x0000FFFF0000FFFFull) \
        : "memory"
template <int NT>
DI void run_a(unsigned kaddr, unsigned vaddr, bf16x8 q, const f32x16& nm, f32x16& o, u32x4 v0) {
    u32x4 v1 = v0;
    f32x16 s0, s1;
    u32x4 kf;
    if constexpr (NT == 8)
        asm volatile(A_RUN_HEAD A_PAIR(1024) A_PAIR(3072) A_PAIR(5120) A_RUN_TAIL(7168) A_RUN_OPS);
    else
        asm volatile(A_RUN_HEAD A_PAIR(1024) A_PAIR(3072) A_PAIR(5120) A_PAIR(7168) A_PAIR(9216) A_PAIR(11264) A_PAIR(13312) A_PAIR(15360)
                     A_PAIR(17408) A_PAIR(19456) A_PAIR(21504) A_RUN_TAIL(23552) A_RUN_OPS);
}

DI void store_o(char* ws, int br, int qtile, int head, int lane, const f32x16& o) {
    const float inv = 1.0f / o[8];
    u32x4 v;
#pragma unroll
    for (int i = 0; i < 4; ++i) v[i] = cvtpk(o[2 * i] * inv, o[2 * i + 1] * inv);
    st16_wt(ws + WS_O + (size_t)br * TB + (size_t)(qtile * 32 + head) * 1024 + lane * 16, v);
}
DI void put_partial(LDSP float* ex, const f32x16& o, float m) {
#pragma unroll
    for (int r = 0; r < 9; ++r) ex[r * 64] = o[r];
    ex[9 * 64] = m;
}
DI void merge_partial(const LDSP float* ex, f32x16& o, float m) {
    const float m1 = ex[9 * 64], mn = fmaxf(m, m1);
    const float s0 = __builtin_amdgcn_exp2f(m - mn), s1 = __builtin_amdgcn_exp2f(m1 - mn);
#pragma unroll
    for (int r = 0; r < 9; ++r) o[r] = o[r] * s0 + ex[r * 64] * s1;
}
DI unsigned l_ex(int j) { return j < 6 ? 98304 + j * 2560 : 148480 + (j - 6) * 2560; }
DI unsigned l_wob(int q) { return q < 2 ? 114688 + q * 16384 : (q - 2) * 16384; }
DI unsigned l_woa(int q) { return 32768 + q * 16384; }
DI unsigned l_red(int br, int g, int oq) { return l_wob(g) + oq * 8192; }
DI void wo_issue(const char* Wo, int nb, int p, unsigned dst, int lane, unsigned lds0) {
    const int r = lane >> 3, sl = (lane & 7) ^ (((r >> 1) & 1) << 2);
    glds16(Wo + (size_t)p * 8192, (unsigned)r * 1024u + nb * 128 + sl * 16, lds0 + dst);
}

template <int SLEEP = 1>
DI void group_sync(LDSP char* ctr, unsigned target, int lane) {
    asm volatile("s_waitcnt lgkmcnt(0)" ::: "memory");
    if (lane == 0) __hip_atomic_fetch_add((LDSP unsigned*)ctr, 1u, __ATOMIC_RELAXED, __HIP_MEMORY_SCOPE_WORKGROUP);
    if ((unsigned)rfl(*(volatile LDSP unsigned*)ctr) < target) {
#pragma unroll 1
        for (int it = 0; it < SPIN_MAX; ++it) {
            __builtin_amdgcn_s_sleep(SLEEP);
            if ((unsigned)rfl(*(volatile LDSP unsigned*)ctr) >= target) break;
        }
    }
    asm volatile("" ::: "memory");
}

template <int SLEEP>
DI void lds_await_count(LDSP char* ctr, unsigned target) {
#pragma unroll 1
    for (int it = 0; it < SPIN_MAX; ++it) {
        if ((unsigned)rfl(*(volatile LDSP unsigned*)ctr) >= target) break;
        __builtin_amdgcn_s_sleep(SLEEP);
    }
    asm volatile("" ::: "memory");
}
struct OutAddr { unsigned wa[2][2]; };
DI OutAddr out_addr(unsigned wlo, unsigned whi, int w, int lane) {
    const int kq = (w >> 1) & 3, h = lane >> 5, g1 = (lane >> 4) & 1, q = (lane & 15) >> 2, p = lane & 3;
    OutAddr a;
#pragma unroll
    for (int j = 0; j < 2; ++j) {
        const int nt = j ^ (kq >> 1);
        const unsigned l = (unsigned)(4 * h + q) * 128u + (((nt * 32 + 16 * (p & 1) + 8 * (g1 ^ (kq & 1)) + 4 * (p >> 1)) * 2) ^ (((q >> 1) & 1) << 6));
        a.wa[0][j] = wlo + l; a.wa[1][j] = whi + l;
    }
    return a;
}
template <bool FIRST>
DI void out_reduce(const f32x16 (&acc)[2], char* out, int br, int qb, int nb, int w, int lane, LDSP char* lds, LDSP char* gate, unsigned gate_n, LDSP char* ctr, unsigned gs PROF_ARG) {
    const int oq = w & 1, kq = (w >> 1) & 3, h = lane >> 5;
    SX(12 + 12 + 6 * (1 - br));
    if (FIRST) {
        asm volatile("s_waitcnt lgkmcnt(0)" ::: "memory");
        if (lane == 0) __hip_atomic_fetch_add((LDSP unsigned*)gate, 1u, __ATOMIC_RELAXED, __HIP_MEMORY_SCOPE_WORKGROUP);
    }
    if (FIRST) lds_await_count<3>(gate, gate_n); else lds_await_count<1>(gate, gate_n);
#pragma unroll
    for (int x = 1; x < 4; ++x) {
        LDSP f32x4* e = (LDSP f32x4*)(lds + l_red(br, kq ^ x, oq) + (x - 1) * 2048) + lane;
        const int j = x >> 1, u = x & 1;
        e[0] = f32x4{acc[j][8 * u], acc[j][8 * u + 1], acc[j][8 * u + 2], acc[j][8 * u + 3]};
        e[64] = f32x4{acc[j][8 * u + 4], acc[j][8 * u + 5], acc[j][8 * u + 6], acc[j][8 * u + 7]};
    }
    if (br == 0) SX(36);
    if (FIRST) group_sync<3>(ctr, gs + 8, lane); else group_sync<1>(ctr, gs + 8, lane);
    if (br == 0) SX(37);
    float r[8];
#pragma unroll
    for (int x = 0; x < 8; ++x) r[x] = acc[0][x];
#pragma unroll
    for (int k = 0; k < 3; ++k) {
        const LDSP f32x4* e = (const LDSP f32x4*)(lds + l_red(br, kq, oq) + k * 2048) + lane;
        const f32x4 a = e[0], b = e[64];
#pragma unroll
        for (int x = 0; x < 4; ++x) { r[x] += a[x]; r[4 + x] += b[x]; }
    }
    if (FIRST) {
        asm volatile("s_waitcnt lgkmcnt(0)" : "+v"(r[0]), "+v"(r[1]), "+v"(r[2]), "+v"(r[3]), "+v"(r[4]), "+v"(r[5]), "+v"(r[6]), "+v"(r[7]) :: "memory");
        if (lane == 0) __hip_atomic_fetch_add((LDSP unsigned*)gate, 1u, __ATOMIC_RELAXED, __HIP_MEMORY_SCOPE_WORKGROUP);
    }
    u32x4 v;
#pragma unroll
    for (int i = 0; i < 4; ++i) v[i] = cvtpk(r[2 * i], r[2 * i + 1]);
    *(u32x4*)(out + (size_t)((qb * 2 + oq) * 32 + (lane & 31)) * 2048 + br * 1024 + nb * 128 + (kq >> 1) * 64 + h * 32 + (kq & 1) * 16) = v;
    SX(12 + 13 + 6 * (1 - br));
}
template <bool FIRST>
DI void out_tile(u32x4 (&of)[8], unsigned wlo, unsigned whi, char* out, int br, int qb, int nb, int w, LDSP char* lds, LDSP char* gate, unsigned gate_n, LDSP char* ctr, unsigned gs PROF_ARG) {
    const int lane = fresh_lane();
    const OutAddr a = out_addr(wlo, whi, w, lane);
    f32x16 acc[2] = {f32x16{}, f32x16{}};
    bf16x8 wf[2][2];
#pragma unroll
    for (int j = 0; j < 2; ++j) wf[0][j] = cat44(lds_tr(lds + a.wa[0][j]), lds_tr(lds + a.wa[0][j] + 1024));
#pragma unroll
    for (int i = 0; i < 8; ++i) {
        if (i < 7) {
#pragma unroll
            for (int j = 0; j < 2; ++j) wf[(i + 1) & 1][j] = cat44(lds_tr(lds + a.wa[(i + 1) >> 2][j] + (i + 1) * 2048), lds_tr(lds + a.wa[(i + 1) >> 2][j] + (i + 1) * 2048 + 1024));
        }
        asm volatile("s_waitcnt vmcnt(%1)" : "+v"(of[i]) : "i"(7 - i) : "memory");
#pragma unroll
        for (int j = 0; j < 2; ++j) acc[j] = mfma(wf[i & 1][j], __builtin_bit_cast(bf16x8, of[i]), acc[j]);
    }
    out_reduce<FIRST>(acc, out, br, qb, nb, w, lane, lds, gate, gate_n, ctr, gs PROF_PASS);
}
#define OP_LOAD(I, B, OFF) "s_bitcmp1_b32 %[rdy], " #I "\n\ts_cbranch_scc0 Lnl" #I "_%=\n\tglobal_load_dwordx4 v[64+4*" #I ":67+4*" #I "], %[lo], " B " offset:" #OFF " sc1\n\tLnl" #I "_%=:\n\t"
#define OP_MUL(I) "s_bitcmp1_b32 %[rdy], " #I "\n\ts_cbranch_scc0 Lnm" #I "_%=\n\t" \
    "ds_read_b64_tr_b16 v[96:97], %[w0] offset:" #I "*2048\n\tds_read_b64_tr_b16 v[98:99], %[w0] offset:" #I "*2048+1024\n\t" \
    "ds_read_b64_tr_b16 v[100:101], %[w1] offset:" #I "*2048\n\tds_read_b64_tr_b16 v[102:103], %[w1] offset:" #I "*2048+1024\n\t" \
    "s_waitcnt lgkmcnt(2)\n\tv_mfma_f32_32x32x16_bf16 v[32:47], v[96:99], v[64+4*" #I ":67+4*" #I "], v[32:47]\n\t" \
    "s_waitcnt lgkmcnt(0)\n\tv_mfma_f32_32x32x16_bf16 v[48:63], v[100:103], v[64+4*" #I ":67+4*" #I "], v[48:63]\n\tLnm" #I "_%=:\n\t"
DI void out_progressive(f32x16& a0, f32x16& a1, const char* Of, unsigned lo, unsigned w0, unsigned w1, unsigned mask_addr, int shift) {
    unsigned done, rdy, cnt;
    u32x4 t0, t1, t2, t3, t4, t5, t6, t7, t8, t9;
    asm volatile(
        "s_mov_b32 %[done], 0\n\t"
        "s_mov_b32 %[cnt], 0x40000\n\t"
        "Lpass_%=:\n\t"
        "ds_read_b32 v96, %[ma]\n\t"
        "s_waitcnt lgkmcnt(0)\n\t"
        "v_readfirstlane_b32 %[rdy], v96\n\t"
        "s_nop 0\n\t"
        "s_lshr_b32 %[rdy], %[rdy], %[sh]\n\t"
        "s_and_b32 %[rdy], %[rdy], 0xff\n\t"
        "s_andn2_b32 %[rdy], %[rdy], %[done]\n\t"
        "s_cbranch_scc1 Lgo_%=\n\t"
        "s_sleep 1\n\t"
        "s_sub_u32 %[cnt], %[cnt], 1\n\t"
        "s_cmp_lg_u32 %[cnt], 0\n\t"
        "s_cbranch_scc1 Lpass_%=\n\t"
        "s_andn2_b32 %[rdy], 0xff, %[done]\n\t"
        "Lgo_%=:\n\t"
        OP_LOAD(0, "%[b0]", 0) OP_LOAD(1, "%[b0]", 1024) OP_LOAD(2, "%[b0]", 2048) OP_LOAD(3, "%[b0]", 3072)
        OP_LOAD(4, "%[b1]", 0) OP_LOAD(5, "%[b1]", 1024) OP_LOAD(6, "%[b1]", 2048) OP_LOAD(7, "%[b1]", 3072)
        "s_waitcnt vmcnt(0)\n\t"
        OP_MUL(0) OP_MUL(1) OP_MUL(2) OP_MUL(3) OP_MUL(4) OP_MUL(5) OP_MUL(6) OP_MUL(7)
        "s_or_b32 %[done], %[done], %[rdy]\n\t"
        "s_cmp_lg_u32 %[done], 0xff\n\t"
        "s_cbranch_scc1 Lpass_%=\n\t"
        "s_nop 15\n\t"
        : "+{v[32:47]}"(a0), "+{v[48:63]}"(a1), "=&{v[64:67]}"(t0), "=&{v[68:71]}"(t1), "=&{v[72:75]}"(t2), "=&{v[76:79]}"(t3), "=&{v[80:83]}"(t4), "=&{v[84:87]}"(t5),
          "=&{v[88:91]}"(t6), "=&{v[92:95]}"(t7), "=&{v[96:99]}"(t8), "=&{v[100:103]}"(t9), [done] "=&s"(done), [rdy] "=&s"(rdy), [cnt] "=&s"(cnt)
        : [lo] "v"(lo), [b0] "s"(Of), [b1] "s"(Of + 4096), [w0] "v"(w0), [w1] "v"(w1), [ma] "v"(mask_addr), [sh] "s"(shift)
        : "memory", "scc");
}
DI void poll_mask(const char* p, unsigned token, LDSP char* word) {
    unsigned v0, v1, v2, v3, known = 0;
#define POLL_ISSUE(V) asm volatile("global_load_dword %0, %1, off sc1" : "=v"(V) : "v"(p) : "memory")
#define POLL_STEP(V) { asm volatile("s_waitcnt vmcnt(3)" : "+v"(V) :: "memory"); const unsigned m = known | (unsigned)__ballot(V == token); \
        if (m != known) { known = m; lds_signal(word, m); if (m == ~0u) break; } POLL_ISSUE(V); __builtin_amdgcn_s_sleep(1); }
    POLL_ISSUE(v0); __builtin_amdgcn_s_sleep(1); POLL_ISSUE(v1); __builtin_amdgcn_s_sleep(1); POLL_ISSUE(v2); __builtin_amdgcn_s_sleep(1); POLL_ISSUE(v3);
#pragma unroll 1
    for (int it = 0; it < SPIN_MAX; ++it) {
        POLL_STEP(v0) POLL_STEP(v1) POLL_STEP(v2) POLL_STEP(v3)
    }
    asm volatile("s_waitcnt vmcnt(0)" : "+v"(v0), "+v"(v1), "+v"(v2), "+v"(v3) :: "memory");
#undef POLL_ISSUE
#undef POLL_STEP
}
DI void load_o(u32x4 (&of)[8], const char* ws, int br, int qb, int w, int lane) {
    const char* Of = ws + WS_O + (size_t)br * TB + (size_t)(qb * 2 + (w & 1)) * 32768 + ((w >> 1) & 3) * 8192;
#pragma unroll
    for (int i = 0; i < 8; ++i) of[i] = ld16_dev(Of + i * 1024, lane * 16);
}

DI void phase_attn(char* ws, const char* WoA, const char* WoB, char* out, int qb, int nb, LDSP char* lds, unsigned token PROF_ARG) {
    const unsigned lds0 = (unsigned)(uintptr_t)lds;
    int lane = fresh_lane();
    const int w = rfl(threadIdx.x >> 6);
    const int bid = blockIdx.x, head = (bid & 7) * 4 + (bid >> 6), jb = (bid >> 3) & 7;
    const int qt = w & 7, half = w >> 3, Qt = jb * 8 + qt;
    const char* KA = ws + WS_K + (size_t)head * 65536;
    const char* VA = ws + WS_V + (size_t)head * 65536;
    const char* qp = ws + WS_Q + (size_t)(head * 64 + Qt) * 1024;
    if (half == 1) {
        __builtin_amdgcn_s_setprio(3);
        lds_await<1>(lds + L_C + 8, ~token);
#pragma unroll
        for (int i = 0; i < 6; ++i) {
            const int h = i & 1, t = (i >> 1) * 8 + qt;
            glds16_dev(KA + (size_t)(h * 32 + 8 + t) * 1024, lane * 16, lds0 + h * 49152 + t * 1024);
            glds16_dev(VA + (size_t)(h * 32 + 8 + t) * 1024, lane * 16, lds0 + h * 49152 + 24576 + t * 1024);
        }
    }
    lds_await_count<1>(lds + L_C + 12, 4);
    SX(7);
    __builtin_amdgcn_s_setprio(1);
    const bf16x8 qa = lds_frag(lds + L_QA + qt * 1024 + lane * 16);
    f32x16 oa = f32x16{};
    float ma = xhalf_max(max16(mfma(lds_frag(lds + L_LEAD + half * 16384 + lane * 16), qa, f32x16{})));
    const f32x16 nma = bcast16(-ma);
    run_a<8>(lds0 + L_LEAD + half * 16384 + lane * 16, lds0 + L_LEAD + half * 16384 + 8192 + (lane >> 5) * 256 + (lane & 15) * 16, qa, nma, oa, v_init(lane));
    SX(8);
    asm volatile("s_waitcnt vmcnt(0)" ::: "memory");
    if (w >= 4 && w < 8) {
        const int xcd = bid & 7, idx = bid >> 3, tb = (xcd & 3) * 4 + (idx & 3), cb = (xcd >> 2) * 8 + (idx >> 2);
        publish(lds + L_C + 4, 3, ws + F_G + (128 + cb) * 128 + tb * 4, 8, 2048, token, lane);
    }
    asm volatile("s_barrier" ::: "memory");
    SX(12 + 0);
    lane = fresh_lane();
    if (w == 15) {
        __builtin_amdgcn_s_setprio(3);
        poll_flags(b_flag(ws, head, jb, lane), token);
        stage_window<16>(ws, lds0, head, jb, 0, lane);
        asm volatile("s_waitcnt vmcnt(0)" ::: "memory");
        lds_signal(lds + L_T + 32, 1);
        lane = fresh_lane();
    }
    run_a<24>(lds0 + half * 49152 + lane * 16, lds0 + half * 49152 + 24576 + (lane >> 5) * 256 + (lane & 15) * 16, qa, nma, oa, v_init(lane));
    if (half) __builtin_amdgcn_s_setprio(3); else __builtin_amdgcn_s_setprio(2);
    lane = fresh_lane();
    LDSP float* ex = (LDSP float*)(lds + l_ex(qt)) + lane;
    SX(12 + 1);
    if (__any(unsafe_state(oa))) safe_tiles(KA, VA, half * 32, 32, 1, 0, 0, qa, lane, oa, ma);
    const int kq = (w >> 1) & 3;
    if (half == 1) {
        if (lane == 0) __hip_atomic_fetch_add((LDSP unsigned*)(lds + L_T + 56), 1u, __ATOMIC_RELAXED, __HIP_MEMORY_SCOPE_WORKGROUP);
        lds_await<1>(lds + L_T + 4 * qt, 0);
        SX(12 + 2);
        merge_partial(ex, oa, ma);
        store_o(ws, 0, Qt, head, lane, oa);
        publish(lds + L_T + 44, 7, ws + F_O + jb * 128 + head * 4, 4, 1024, token, lane);
        SX(12 + 4);
    } else {
        put_partial(ex, oa, ma);
        lds_signal(lds + L_T + 4 * qt, 1);
    }
    if (half == 1) {
        lds_await_count<1>(lds + L_T + 60, 8);
        const OutAddr a = out_addr(l_woa(kq), l_woa(kq), w, lane);
        const char* Of = ws + WS_O + (size_t)(qb * 2 + (w & 1)) * 32768 + kq * 8192;
        f32x16 acc[2] = {f32x16{}, f32x16{}};
        out_progressive(acc[0], acc[1], uni(Of), lane * 16, lds0 + a.wa[0][0], lds0 + a.wa[0][1], lds0 + L_T + 72, 8 * kq);
        SX(33);
        out_reduce<false>(acc, out, 0, qb, nb, w, lane, lds, lds + L_T + 68, 16, lds + L_T + 64, 0 PROF_PASS);
        return;
    }
    {
        lds_await<2>(lds + L_T + 32, 0);
        u32x4 qbr = ld16_dev(qp + TB, lane * 16);
        asm volatile("s_waitcnt vmcnt(0)" : "+v"(qbr) :: "memory");
        SX(12 + 3);
        const bf16x8 qbf = __builtin_bit_cast(bf16x8, qbr);
        const int lo = lane * 16, z = (lane & 31) - 4 * (lane >> 5);
        const unsigned vl = (lane >> 5) * 256 + (lane & 15) * 16;
        const int t0 = qt + 8, nt = jb ? 9 : qt + 1;
        f32x16 o = f32x16{};
        f32x16 s = mfma(lds_frag(lds + L_BK + t0 * 1024 + lo), qbf, f32x16{});
        mask_tile(s, 1, z);
        float m = max16(s);
        if (nt > 1) m = fmaxf(m, max16(mfma(lds_frag(lds + L_BK + (t0 - 1) * 1024 + lo), qbf, f32x16{})));
        m = xhalf_max(m);
        const f32x16 nm = bcast16(-m);
        if (jb) {
#pragma unroll
            for (int r = 0; r < 16; ++r) s[r] -= m;
            f32x16 cfar = nm;
            mask_tile(cfar, 2, z);
            run_b9(lds0 + L_BK + qt * 1024 + lo, lds0 + L_BV + qt * 1024 + vl, qbf, s, nm, cfar, o, v_init(lane));
        } else {
            u32x4 v0 = v_init(lane), v1 = v0;
            for (int i = 0; i < nt; ++i) {
                f32x16 p = mfma(lds_frag(lds + L_BK + (t0 - i) * 1024 + lo), qbf, nm);
                if (i == 0) mask_tile(p, 1, z);
#pragma unroll
                for (int r = 0; r < 16; ++r) p[r] = __builtin_amdgcn_exp2f(p[r]);
                load_v(v0, v1, lds0 + L_BV + (t0 - i) * 1024 + vl);
                pv_regs(p, o, v0, v1);
            }
        }
        lane = fresh_lane();
        if (__any(unsafe_state(o)))
            safe_tiles(ws + WS_K + TB + (size_t)head * 65536, ws + WS_V + TB + (size_t)head * 65536, Qt, nt, -1, 1, jb ? 2 : 0, qbf, lane, o, m);
        SX(12 + 5);
        store_o(ws, 1, Qt, head, lane, o);
        publish(lds + L_T + 40, 7, ws + F_O + (32 + jb) * 128 + head * 4, 4, 1024, token, lane);
    }
    SX(12 + 6);
    group_sync<8>(lds + L_T + 36, 8, lane);
#pragma unroll
    for (int i = 0; i < 8; ++i) { const int p = w * 8 + i; wo_issue(WoB, nb, p, l_wob(p >> 4) + (p & 15) * 1024, lane, lds0); }
#pragma unroll
    for (int i = 0; i < 2; ++i) { const int p = w * 2 + i; wo_issue(WoA, nb, p, l_woa(0) + p * 1024, lane, lds0); }
    if (w == 0) {
        asm volatile("s_waitcnt vmcnt(0)" ::: "memory");
        poll_flags(ws + F_O + ((4 + (nb & 3)) * 8 + (qb >> 2)) * 128 + (lane & 31) * 4, token);
        lds_signal(lds + L_T + 48, token);
    } else lds_await<6>(lds + L_T + 48, ~token);
    u32x4 of[8];
    load_o(of, ws, 1, qb, w, lane);
    SX(12 + 7);
    asm volatile("s_waitcnt vmcnt(0)" : "+v"(of[0]), "+v"(of[1]), "+v"(of[2]), "+v"(of[3]), "+v"(of[4]), "+v"(of[5]), "+v"(of[6]), "+v"(of[7]) :: "memory");
    group_sync<3>(lds + L_T + 36, 16, lane);
    {
        const OutAddr a = out_addr(l_wob(kq), l_wob(kq), w, lane);
        f32x16 acc[2] = {f32x16{}, f32x16{}};
#pragma unroll
        for (int i = 0; i < 8; ++i)
#pragma unroll
            for (int j = 0; j < 2; ++j)
                acc[j] = mfma(cat44(lds_tr(lds + a.wa[0][j] + i * 2048), lds_tr(lds + a.wa[0][j] + i * 2048 + 1024)), __builtin_bit_cast(bf16x8, of[i]), acc[j]);
        out_reduce<true>(acc, out, 1, qb, nb, w, lane, lds, lds + L_T + 68, 8, lds + L_T + 36, 16 PROF_PASS);
    }
    lds_await_count<6>(lds + L_T + 56, 8);
    SX(12 + 14);
    lane = fresh_lane();
    if (w) {
#pragma unroll
        for (int i = 0; i < 7; ++i) { int p = 16 + (w - 1) * 7 + i; if (p > 63) p = 63; wo_issue(WoA, nb, p, l_woa(0) + p * 1024, lane, lds0); }
    }
    asm volatile("s_waitcnt vmcnt(0)" ::: "memory");
    if (lane == 0) __hip_atomic_fetch_add((LDSP unsigned*)(lds + L_T + 60), 1u, __ATOMIC_RELAXED, __HIP_MEMORY_SCOPE_WORKGROUP);
    if (w == 0) {
        const char* fl = ws + F_O + ((nb & 3) * 8 + (qb >> 2)) * 128 + (lane & 31) * 4;
        poll_mask(fl, token, lds + L_T + 72);
    }
}

__global__ __launch_bounds__(1024) void k_main(const char* xA, const char* wA, const char* oA, const char* xB, const char* wB, const char* oB, char* ws, char* out) {
    extern __shared__ __attribute__((aligned(16))) char smem[];
    LDSP char* lds = (LDSP char*)smem;
    const int bid = blockIdx.x, xcd = bid & 7, idx = bid >> 3;
    RSTAMP(0);
    const unsigned token = rfl(ld_flag(ws + F_EPOCH)) + 1;
    phase_qkv(xA, wA, oA, xB, wB, oB, ws, lds, token PROF_PASS);
    phase_attn(ws, oA, oB, out, xcd * 4 + (idx & 3), idx >> 2, lds, token PROF_PASS);
    if (bid == 0 && threadIdx.x < 64) {
        const int lane = threadIdx.x;
#pragma unroll
        for (int i = 0; i < 4; ++i) poll_flags(ws + F_G + (i * 4 + (lane >> 4)) * 128 + (lane & 15) * 4, token);
        if (lane == 0) st_flag(ws + F_EPOCH, token);
    }
    RSTAMP(1);
}

extern "C" void kernel_launch(void* const* d_in, const int* in_sizes, int n_in, void* d_out, int out_size, void* d_ws, size_t ws_size,
                              hipStream_t stream) {
    const char* xA = (const char*)d_in[0];
    const char* wA = (const char*)d_in[1];
    const char* oA = (const char*)d_in[2];
    const char* xB = (const char*)d_in[3];
    const char* wB = (const char*)d_in[4];
    const char* oB = (const char*)d_in[5];
    char* ws = (char*)d_ws;
    char* out = (char*)d_out;
    (void)hipFuncSetAttribute((const void*)k_main, hipFuncAttributeMaxDynamicSharedMemorySize, 163840);
    k_main<<<256, 1024, 163840, stream>>>(xA, wA, oA, xB, wB, oB, ws, out);
}
```

```cpp
#include <hip/hip_runtime.h>
#include <hip/hip_bf16.h>
#include <stdint.h>

typedef __bf16 bf16x8 __attribute__((ext_vector_type(8)));
typedef __bf16 bf16x2 __attribute__((ext_vector_type(2)));
typedef short s16x4 __attribute__((ext_vector_type(4)));
typedef short s16x8 __attribute__((ext_vector_type(8)));
typedef float f32x16 __attribute__((ext_vector_type(16)));
typedef float f32x4 __attribute__((ext_vector_type(4)));
typedef float f32x2 __attribute__((ext_vector_type(2)));
typedef unsigned u32x4 __attribute__((ext_vector_type(4)));
#define LDSP __attribute__((address_space(3)))
#define DI __device__ __forceinline__

constexpr size_t TB = 2u << 20;
constexpr size_t WS_Q = 0, WS_K = 2 * TB, WS_V = 4 * TB, WS_O = 6 * TB;
constexpr float QSCALE = 0.25f * 1.4426950408889634f;

DI unsigned cvtpk(float lo, float hi) {
    f32x2 v = {lo, hi};
    bf16x2 b = __builtin_convertvector(v, bf16x2);
    return __builtin_bit_cast(unsigned, b);
}
DI f32x16 mfma(bf16x8 a, bf16x8 b, f32x16 c) { return __builtin_amdgcn_mfma_f32_32x32x16_bf16(a, b, c, 0, 0, 0); }
DI int rfl(int x) { return __builtin_amdgcn_readfirstlane(x); }
DI const char* uni(const void* p) {
    const uint64_t a = (uint64_t)p;
    return (const char*)(((uint64_t)(unsigned)rfl((int)(a >> 32)) << 32) | (unsigned)rfl((int)a));
}
DI void glds16(const void* sbase, unsigned voff, unsigned lds_base) {
    unsigned sv;
    asm volatile("s_mov_b32 %0, m0\n\ts_mov_b32 m0, %3\n\ts_nop 2\n\tglobal_load_lds_dwordx4 %1, %2\n\ts_mov_b32 m0, %0"
                 : "=&s"(sv) : "v"(voff), "s"(uni(sbase)), "s"(rfl((int)lds_base)) : "memory");
}
DI void glds16_dev(const void* sbase, unsigned voff, unsigned lds_base) {
    unsigned sv;
    asm volatile("s_mov_b32 %0, m0\n\ts_mov_b32 m0, %3\n\ts_nop 2\n\tglobal_load_lds_dwordx4 %1, %2 sc1\n\ts_mov_b32 m0, %0"
                 : "=&s"(sv) : "v"(voff), "s"(uni(sbase)), "s"(rfl((int)lds_base)) : "memory");
}
DI int fresh_lane() { int l; asm volatile("v_mbcnt_lo_u32_b32 %0, -1, 0\n\tv_mbcnt_hi_u32_b32 %0, -1, %0" : "=v"(l)); return l; }
DI bf16x8 lds_frag(const LDSP char* p) { return *(const LDSP bf16x8*)p; }
DI s16x4 lds_tr(const LDSP char* p) { return __builtin_amdgcn_ds_read_tr16_b64_v4i16((LDSP s16x4*)p); }
DI bf16x8 cat44(s16x4 a, s16x4 b) {
    s16x8 r = {a[0], a[1], a[2], a[3], b[0], b[1], b[2], b[3]};
    return __builtin_bit_cast(bf16x8, r);
}
#define WAITV_BAR(N) asm volatile("s_waitcnt vmcnt(" #N ")\n\ts_barrier" ::: "memory")

constexpr size_t WS_F = 8 * TB;
constexpr size_t F_EPOCH = WS_F, F_G = WS_F + 4096, F_O = F_G + 32768;
constexpr int SPIN_MAX = 1 << 18;
typedef __attribute__((address_space(1))) unsigned gu32;
DI unsigned ld_flag(const char* p) { return __hip_atomic_load((gu32*)p, __ATOMIC_RELAXED, __HIP_MEMORY_SCOPE_AGENT); }
DI void st_flag(char* p, unsigned v) { __hip_atomic_store((gu32*)p, v, __ATOMIC_RELAXED, __HIP_MEMORY_SCOPE_AGENT); }
DI void st16_wt(char* p, u32x4 v) { asm volatile("global_store_dwordx4 %0, %1, off sc1\n\ts_nop 1" :: "v"(p), "v"(v) : "memory"); }
DI u32x4 ld16_dev(const char* sbase, unsigned voff) {
    u32x4 r;
    asm volatile("s_nop 4\n\tglobal_load_dwordx4 %0, %1, %2 sc1" : "=&v"(r) : "v"(voff), "s"(uni(sbase)) : "memory");
    return r;
}
DI void poll_flags(const char* p, unsigned token) {
    unsigned v0, v1, v2, v3;
#define POLL_ISSUE(V) asm volatile("global_load_dword %0, %1, off sc1" : "=v"(V) : "v"(p) : "memory")
#define POLL_STEP(V) asm volatile("s_waitcnt vmcnt(3)" : "+v"(V) :: "memory"); if (__all(V == token)) break; POLL_ISSUE(V); __builtin_amdgcn_s_sleep(2);
    POLL_ISSUE(v0); __builtin_amdgcn_s_sleep(2); POLL_ISSUE(v1); __builtin_amdgcn_s_sleep(2); POLL_ISSUE(v2); __builtin_amdgcn_s_sleep(2); POLL_ISSUE(v3);
#pragma unroll 1
    for (int it = 0; it < SPIN_MAX; ++it) {
        POLL_STEP(v0) POLL_STEP(v1) POLL_STEP(v2) POLL_STEP(v3)
    }
    asm volatile("s_waitcnt vmcnt(0)" : "+v"(v0), "+v"(v1), "+v"(v2), "+v"(v3) :: "memory");
#undef POLL_ISSUE
#undef POLL_STEP
}
DI const char* gflag_addr(const char* ws, int br, int head, int jb, int type, int tb) {
    return ws + F_G + ((br * 8 + jb) * 16 + (type * 512 + head * 16) / 96) * 128 + tb * 4;
}
DI const char* a_flag(const char* ws, int head, int jb, int lane) {
    const int type = lane < 16 ? 1 : (lane < 32 ? 2 : 0);
    return gflag_addr(ws, 0, head, jb, type, lane < 32 ? (lane & 15) : 2 * jb + (lane & 1));
}
DI const char* b_flag(const char* ws, int head, int jb, int lane) {
    const int type = lane < 16 ? 1 : (lane < 32 ? 2 : 0);
    int tb = lane < 32 ? 2 * jb - 2 + (lane & 3) : 2 * jb + (lane & 1);
    if (tb < 0) tb = 0;
    return gflag_addr(ws, 1, head, jb, type, tb);
}

constexpr unsigned L_LEAD = 114688, L_BK = 114688, L_BV = 131072, L_C = 147456, L_QA = 153600;
constexpr unsigned L_T = L_C + 64;
DI void stage_lead(const char* ws, unsigned lds0, int head, int jb, int w, int lane) {
    const char* KA = ws + WS_K + (size_t)head * 65536;
    const char* VA = ws + WS_V + (size_t)head * 65536;
#pragma unroll
    for (int i = 0; i < 4; ++i) {
        const int e = w * 4 + i, h = e >> 3, t = e & 7;
        glds16_dev(KA + (size_t)(h * 32 + t) * 1024, lane * 16, lds0 + L_LEAD + h * 16384 + t * 1024);
        glds16_dev(VA + (size_t)(h * 32 + t) * 1024, lane * 16, lds0 + L_LEAD + h * 16384 + 8192 + t * 1024);
    }
#pragma unroll
    for (int i = 0; i < 2; ++i) glds16_dev(ws + WS_Q + (size_t)(head * 64 + jb * 8 + w * 2 + i) * 1024, lane * 16, lds0 + L_QA + (w * 2 + i) * 1024);
}
template <int N>
DI void stage_window(const char* ws, unsigned lds0, int head, int jb, int first, int lane) {
    const char* KB = ws + WS_K + TB + (size_t)head * 65536;
    const char* VB = ws + WS_V + TB + (size_t)head * 65536;
#pragma unroll
    for (int i = 0; i < N; ++i) {
        int t = jb * 8 - 8 + first + i; if (t < 0) t = 0;
        glds16_dev(KB + (size_t)t * 1024, lane * 16, lds0 + L_BK + (first + i) * 1024);
        glds16_dev(VB + (size_t)t * 1024, lane * 16, lds0 + L_BV + (first + i) * 1024);
    }
}

constexpr int G_STAGE = 28672, G_XB = 16384;

#define G_RD_X(NX, XA, XO)        "ds_read_b128 v[" #NX ":" #NX "+3], v" #XA " offset:" XO "\n\t"
#define G_RD_W(NW, WA, WO, C)     "ds_read_b64_tr_b16 v[" #NW ":" #NW "+1], v" #WA " offset:" WO "+" #C "*64\n\t" \
                                  "ds_read_b64_tr_b16 v[" #NW "+2:" #NW "+3], v" #WA " offset:" WO "+768+" #C "*64\n\t"
#define G_MF_T(ACC, X, W)         "v_mfma_f32_32x32x16_bf16 v[" #ACC ":" #ACC "+15], v[" #W ":" #W "+3], v[" #X ":" #X "+3], v[" #ACC ":" #ACC "+15]\n\t"
#define G_MF_C(ACC, X, W)         "v_mfma_f32_32x32x16_bf16 v[" #ACC ":" #ACC "+15], v[" #X ":" #X "+3], v[" #W ":" #W "+3], v[" #ACC ":" #ACC "+15]\n\t"
#define G_STEP(M0, M1, M2, PX, PW0, PW1, PW2, NX, NW0, NW1, NW2, XA, XO, WA, WO, PRE, POST) \
    PRE G_RD_X(NX, XA, XO) G_RD_W(NW0, WA, WO, 0) POST \
    M0(32, PX, PW0) G_RD_W(NW1, WA, WO, 1) M1(48, PX, PW1) G_RD_W(NW2, WA, WO, 2) M2(64, PX, PW2)
#define G_STEP_A(M0, M1, M2, XA, XO, WA, WO, PRE, POST) G_STEP(M0, M1, M2, 80, 84, 88, 92, 96, 100, 104, 108, XA, XO, WA, WO, PRE, POST)
#define G_STEP_B(M0, M1, M2, XA, XO, WA, WO, PRE, POST) G_STEP(M0, M1, M2, 96, 100, 104, 108, 80, 84, 88, 92, XA, XO, WA, WO, PRE, POST)
#define G_W3 "s_waitcnt lgkmcnt(3)\n\t"
#define G_STAGE_ASM(M0, M1, M2, X1, X2, X3, SO, WA, NX0, NSO, NWA) \
    G_STEP_A(M0, M1, M2, X1, SO, WA, SO "+16384+3072", "", G_W3) \
    G_STEP_B(M0, M1, M2, X2, SO, WA, SO "+16384+6144", "", G_W3) \
    G_STEP_A(M0, M1, M2, X3, SO, WA, SO "+16384+9216", "", G_W3) \
    G_STEP_B(M0, M1, M2, NX0, NSO, NWA, NSO "+16384", "s_waitcnt lgkmcnt(0)\n\ts_barrier\n\t", "")
#define G_ROUND_ASM(M0, M1, M2) \
    "s_mov_b32 %[cnt], 2\n\t" \
    "1:\n\t" \
    G_STAGE_ASM(M0, M1, M2, 113, 114, 115, "0", 120, 112, "28672", 120) \
    G_STAGE_ASM(M0, M1, M2, 113, 114, 115, "28672", 120, 116, "0", 121) \
    G_STAGE_ASM(M0, M1, M2, 117, 118, 119, "0", 121, 116, "28672", 121) \
    G_STAGE_ASM(M0, M1, M2, 117, 118, 119, "28672", 121, 112, "0", 120) \
    "s_sub_u32 %[cnt], %[cnt], 1\n\t" \
    "s_cmp_lg_u32 %[cnt], 0\n\t" \
    "s_cbranch_scc1 1b\n\t" \
    "s_waitcnt lgkmcnt(0)\n\t" \
    "s_nop 15\n\t"

struct GFrag { u32x4 x, w0, w1, w2; };
template <int VMASK>
DI void g_round(f32x16& a0, f32x16& a1, f32x16& a2, GFrag& f, u32x4 xa, u32x4 xb, unsigned wa, unsigned wb) {
    u32x4 t0, t1, t2, t3;
    int cnt;
#define G_ROUND_CALL(M0, M1, M2) \
    asm volatile(G_ROUND_ASM(M0, M1, M2) \
        : "+{v[32:47]}"(a0), "+{v[48:63]}"(a1), "+{v[64:79]}"(a2), "+{v[80:83]}"(f.x), "+{v[84:87]}"(f.w0), "+{v[88:91]}"(f.w1), "+{v[92:95]}"(f.w2), \
          "=&{v[96:99]}"(t0), "=&{v[100:103]}"(t1), "=&{v[104:107]}"(t2), "=&{v[108:111]}"(t3), [cnt] "=&s"(cnt) \
        : "{v[112:115]}"(xa), "{v[116:119]}"(xb), "{v120}"(wa), "{v121}"(wb) : "memory", "scc")
    if constexpr (VMASK == 0) G_ROUND_CALL(G_MF_T, G_MF_T, G_MF_T);
    else if constexpr (VMASK == 4) G_ROUND_CALL(G_MF_T, G_MF_T, G_MF_C);
    else G_ROUND_CALL(G_MF_C, G_MF_C, G_MF_C);
}
template <int VMASK>
DI void g_epilogue_piece(const f32x16& acc, int c, int u, char* ws, int br, int tb, int cb, int w, int lane) {
    const int t32 = lane & 31, h = lane >> 5, tile = tb * 4 + w;
    const int n0 = cb * 96 + c * 32, type = n0 >> 9, hd0 = (n0 & 511) >> 4;
    u32x4 v;
    if ((VMASK >> c) & 1) {
        char* dst = ws + WS_V + (size_t)br * TB + (size_t)((hd0 + (t32 >> 4)) * 64 + tile) * 1024 + h * 256 + (t32 & 15) * 16;
#pragma unroll
        for (int i = 0; i < 4; ++i) v[i] = cvtpk(acc[8 * u + 2 * i], acc[8 * u + 2 * i + 1]);
        st16_wt(dst + u * 512, v);
    } else {
        const float sc = type == 0 ? QSCALE : 1.0f;
        char* dst = ws + (type == 0 ? WS_Q : WS_K) + (size_t)br * TB + (size_t)(hd0 * 64 + tile) * 1024 + lane * 16;
#pragma unroll
        for (int i = 0; i < 4; ++i) v[i] = cvtpk(acc[8 * u + 2 * i] * sc, acc[8 * u + 2 * i + 1] * sc);
        st16_wt(dst + (size_t)u * 65536, v);
    }
}
template <int VMASK>
DI void g_epilogue_tile(const f32x16& acc, int c, char* ws, int br, int tb, int cb, int w, int lane) {
    g_epilogue_piece<VMASK>(acc, c, 0, ws, br, tb, cb, w, lane);
    g_epilogue_piece<VMASK>(acc, c, 1, ws, br, tb, cb, w, lane);
}
#define BAR() asm volatile("s_barrier" ::: "memory")
DI unsigned get_token(LDSP char* lds) { return rfl(*(volatile LDSP unsigned*)(lds + L_C + 28)); }
DI void publish(LDSP char* ctr, unsigned last, char* flag, int nrep, int rep_stride, unsigned token, int lane) {
    asm volatile("s_waitcnt vmcnt(0)" ::: "memory");
    unsigned old = 0;
    if (lane == 0) old = __hip_atomic_fetch_add((LDSP unsigned*)ctr, 1u, __ATOMIC_RELAXED, __HIP_MEMORY_SCOPE_WORKGROUP);
    if ((unsigned)rfl(old) == last && lane < nrep) st_flag(flag + lane * rep_stride, token);
}
DI void lds_signal(LDSP char* word, unsigned v) { *(volatile LDSP unsigned*)word = v; asm volatile("s_waitcnt lgkmcnt(0)" ::: "memory"); }
template <int SLEEP = 1>
DI void lds_await(LDSP char* word, unsigned initial) {
    if ((unsigned)rfl(*(volatile LDSP unsigned*)word) == initial) {
#pragma unroll 1
        for (int it = 0; it < SPIN_MAX; ++it) {
            __builtin_amdgcn_s_sleep(SLEEP);
            if ((unsigned)rfl(*(volatile LDSP unsigned*)word) != initial) break;
        }
    }
    asm volatile("" ::: "memory");
}
template <int VMASK>
DI void g_mfma_wave(char* ws, int round, int tb, int cb, int w, int lane, LDSP char* lds) {
    const unsigned lds0 = (unsigned)(uintptr_t)lds;
    const int br = round;
    const int w4 = w & 3, t32 = lane & 31, h = lane >> 5, f = ((t32 >> 1) & 7) ^ h;
    u32x4 xa, xb;
#pragma unroll
    for (int ks = 0; ks < 4; ++ks) { xa[ks] = lds0 + (unsigned)(w4 * 32 + t32) * 128u + ((f ^ (2 * ks)) * 16); xb[ks] = xa[ks] + 2 * G_STAGE; }
    const unsigned wa = lds0 + (unsigned)(8 * h + ((lane & 15) >> 2)) * 192u + (16 * ((lane >> 4) & 1) + 4 * (lane & 3)) * 2, wb = wa + 2 * G_STAGE;
    GFrag fr;
    BAR();
    const unsigned token = get_token(lds);
    if (round == 1) {
#pragma unroll
        for (int s = 0; s < 9; ++s) BAR();
    }
    asm volatile("ds_read_b128 %0, %4\n\t"
                 "ds_read_b64_tr_b16 v[84:85], %5 offset:16384\n\tds_read_b64_tr_b16 v[86:87], %5 offset:16384+768\n\t"
                 "ds_read_b64_tr_b16 v[88:89], %5 offset:16384+64\n\tds_read_b64_tr_b16 v[90:91], %5 offset:16384+64+768\n\t"
                 "ds_read_b64_tr_b16 v[92:93], %5 offset:16384+128\n\tds_read_b64_tr_b16 v[94:95], %5 offset:16384+128+768\n\t"
                 "s_waitcnt lgkmcnt(0)"
                 : "=&{v[80:83]}"(fr.x), "=&{v[84:87]}"(fr.w0), "=&{v[88:91]}"(fr.w1), "=&{v[92:95]}"(fr.w2) : "v"(xa[0]), "v"(wa) : "memory");
    f32x16 acc[3] = {f32x16{}, f32x16{}, f32x16{}};
    g_round<VMASK>(acc[0], acc[1], acc[2], fr, xa, xb, wa, wb);
    char* flag = ws + F_G + (br * 128 + cb) * 128 + tb * 4;
    if (round == 0) {
#pragma unroll
        for (int c = 0; c < 3; ++c) g_epilogue_tile<VMASK>(acc[c], c, ws, br, tb, cb, w4, lane);
        publish(lds + L_C, 3, flag, 8, 2048, token, lane);
        BAR();
        BAR(); BAR(); BAR(); BAR();
        const int bid = blockIdx.x, head = (bid & 7) * 4 + (bid >> 6), jb = (bid >> 3) & 7;
        const char* fa = a_flag(ws, head, jb, lane);
        unsigned pv1, pv2;
        asm volatile("global_load_dword %0, %1, off sc1" : "=v"(pv1) : "v"(fa) : "memory");
        BAR(); BAR();
        asm volatile("s_waitcnt vmcnt(0)" : "+v"(pv1) :: "memory");
        bool staged = __all(pv1 == token);
        asm volatile("global_load_dword %0, %1, off sc1" : "=v"(pv2) : "v"(fa) : "memory");
        if (staged) stage_lead(ws, lds0, head, jb, w, lane);
        BAR(); BAR();
        asm volatile("s_waitcnt vmcnt(10)" : "+v"(pv2) :: "memory");
        if (!staged) {
            asm volatile("s_waitcnt vmcnt(0)" : "+v"(pv2) :: "memory");
            if (__all(pv2 == token)) { stage_lead(ws, lds0, head, jb, w, lane); staged = true; }
        }
        BAR();
        if (!staged) { poll_flags(fa, token); stage_lead(ws, lds0, head, jb, w, lane); }
        if (w == 0) lds_signal(lds + L_C + 8, token);
        asm volatile("s_waitcnt vmcnt(0)" ::: "memory");
        if (lane == 0) __hip_atomic_fetch_add((LDSP unsigned*)(lds + L_C + 12), 1u, __ATOMIC_RELAXED, __HIP_MEMORY_SCOPE_WORKGROUP);
    } else {
        BAR();
#pragma unroll
        for (int c = 0; c < 3; ++c) g_epilogue_tile<VMASK>(acc[c], c, ws, br, tb, cb, w4, lane);
    }
}
template <int NX>
DI void g_loader_wave(const char* xA, const char* wA, const char* xB, const char* wB, char* ws, int tb, int cb, int rot, int w, int lane, unsigned lds0) {
    const int j = w & 3;
    unsigned go[NX], ll[NX];
#pragma unroll
    for (int i = 0; i < NX; ++i) {
        const int p = j + 4 * i;
        if (NX == 4) {
            const int row = 8 * p + (lane >> 3), sl = (lane & 7) ^ ((row >> 1) & 7);
            go[i] = (unsigned)(tb * 128 + row) * 1024u + sl * 16; ll[i] = p * 1024;
        } else {
            const int o = p * 1024 + lane * 16, k = o / 192;
            go[i] = (unsigned)k * 3072u + cb * 192 + (o - k * 192); ll[i] = G_XB + p * 1024;
        }
    }
    auto issue = [&](int s) {
        const int ks = (s + rot) & 7;
        const char* src = NX == 4 ? (s < 8 ? xA : xB) + ks * 128 : (s < 8 ? wA : wB) + (size_t)ks * 64 * 3072;
        const unsigned slot = lds0 + (s & 3) * G_STAGE;
#pragma unroll
        for (int i = 0; i < NX; ++i) glds16(src, go[i], slot + ll[i]);
    };
    issue(0);
    issue(1);
    if (NX == 4) WAITV_BAR(4); else WAITV_BAR(3);
    issue(2); issue(3);
#pragma unroll
    for (int s = 0; s < 8; ++s) {
        if (s <= 4 || s == 7) { if (NX == 4) WAITV_BAR(8); else WAITV_BAR(6); }
        else { if (NX == 4) WAITV_BAR(4); else WAITV_BAR(3); }
        if (s + 4 < 8) issue(s + 4);
        if (s == 5) issue(8);
        if (s == 6) issue(9);
    }
    if (NX == 4) WAITV_BAR(4); else WAITV_BAR(3);
    issue(10); issue(11);
#pragma unroll
    for (int s = 8; s < 16; ++s) {
        if (s <= 12) { if (NX == 4) WAITV_BAR(8); else WAITV_BAR(6); }
        else if (s == 13) { if (NX == 4) WAITV_BAR(4); else WAITV_BAR(3); }
        else WAITV_BAR(0);
        if (s + 4 < 16) issue(s + 4);
    }
    BAR();
}
DI unsigned phase_qkv(const char* xA, const char* wA, const char* xB, const char* wB, char* ws, LDSP char* lds) {
    const int lane = threadIdx.x & 63, w = rfl(threadIdx.x >> 6);
    const int bid = blockIdx.x, xcd = bid & 7, idx = bid >> 3;
    const int tb = (xcd & 3) * 4 + (idx & 3), cb = (xcd >> 2) * 8 + (idx >> 2);
    const int rot = (idx >> 2) + 2 * (idx & 3);
    const unsigned lds0 = (unsigned)(uintptr_t)lds;
    if (w == 0) {
        const unsigned t0 = rfl(ld_flag(ws + F_EPOCH)) + 1;
        if (lane == 0) {
#pragma unroll
            for (int i = 0; i < 36; ++i) *(LDSP unsigned*)(lds + L_C + 4 * i) = 0;
            *(LDSP unsigned*)(lds + L_C + 8) = ~t0; *(LDSP unsigned*)(lds + L_T + 48) = ~t0; *(LDSP unsigned*)(lds + L_T + 52) = ~t0;
            *(LDSP unsigned*)(lds + L_C + 28) = t0;
        }
        asm volatile("s_waitcnt lgkmcnt(0)" ::: "memory");
    }
    if (w < 8) {
        if (cb < 10) g_mfma_wave<0>(ws, w >> 2, tb, cb, w, lane, lds);
        else if (cb == 10) g_mfma_wave<4>(ws, w >> 2, tb, cb, w, lane, lds);
        else g_mfma_wave<7>(ws, w >> 2, tb, cb, w, lane, lds);
    } else if (w < 12) g_loader_wave<4>(xA, wA, xB, wB, ws, tb, cb, rot, w, lane, lds0);
    else g_loader_wave<3>(xA, wA, xB, wB, ws, tb, cb, rot, w, lane, lds0);
    return get_token(lds);
}

constexpr float SAFE_L = 1.2676506e30f;
constexpr float SAFE_O = 1.3292280e36f;
constexpr float NEG_BIG = -3.0e38f;
constexpr unsigned ONES = 0x3F803F80u;

DI float xhalf_max(float m) {
    auto r = __builtin_amdgcn_permlane32_swap(__float_as_uint(m), __float_as_uint(m), false, false);
    return fmaxf(__uint_as_float(r[0]), __uint_as_float(r[1]));
}
DI float max16(const f32x16& s) {
    float a = fmaxf(fmaxf(s[0], s[1]), s[2]), b = fmaxf(fmaxf(s[3], s[4]), s[5]);
    a = fmaxf(fmaxf(a, s[6]), s[7]); b = fmaxf(fmaxf(b, s[8]), s[9]);
    a = fmaxf(fmaxf(a, s[10]), s[11]); b = fmaxf(fmaxf(b, s[12]), s[13]);
    a = fmaxf(fmaxf(a, s[14]), s[15]);
    return fmaxf(a, b);
}
DI f32x16 bcast16(float v, int lane) { return __builtin_amdgcn_mfma_f32_32x32x2f32(lane < 32 ? 1.0f : 0.0f, v, f32x16{}, 0, 0, 0); }
DI u32x4 v_init(int lane) { const unsigned x = ((lane & 31) == 16 || (lane & 31) == 20) ? ONES : 0u; return u32x4{x, x, x, x}; }
DI void load_v(u32x4& v0, u32x4& v1, unsigned addr) {
    asm volatile("s_mov_b64 exec, %3\n\tds_read_b128 %0, %2\n\tds_read_b128 %1, %2 offset:512\n\ts_mov_b64 exec, -1\n\ts_waitcnt lgkmcnt(0)"
                 : "+v"(v0), "+v"(v1) : "v"(addr), "s"(0x0000FFFF0000FFFFull) : "memory");
}
DI void pv_regs(const f32x16& p, f32x16& o, const u32x4& v0, const u32x4& v1) {
    u32x4 p0, p1;
#pragma unroll
    for (int i = 0; i < 4; ++i) { p0[i] = cvtpk(p[2 * i], p[2 * i + 1]); p1[i] = cvtpk(p[8 + 2 * i], p[9 + 2 * i]); }
    o = mfma(__builtin_bit_cast(bf16x8, v0), __builtin_bit_cast(bf16x8, p0), o);
    o = mfma(__builtin_bit_cast(bf16x8, v1), __builtin_bit_cast(bf16x8, p1), o);
}
DI void mask_tile(f32x16& s, int mode, int z) {
    if (mode == 0) return;
#pragma unroll
    for (int r = 0; r < 16; ++r) {
        const int cr = (r & 3) + 8 * (r >> 2);
        const bool keep = mode == 1 ? (cr <= z) : (cr > z);
        s[r] = keep ? s[r] : -INFINITY;
    }
}
DI bool unsafe_state(const f32x16& o) {
    const float t = ((fabsf(o[0]) + fabsf(o[1])) + (fabsf(o[2]) + fabsf(o[3]))) + ((fabsf(o[4]) + fabsf(o[5])) + (fabsf(o[6]) + fabsf(o[7])));
    return !(t < SAFE_O) || !(o[8] < SAFE_L);
}
DI void safe_tiles(const char* Kg, const char* Vg, int t0, int n, int step, int first_mode, int last_mode, bf16x8 q, int lane, f32x16& o, float& m) {
    const int z = (lane & 31) - 4 * (lane >> 5);
    const unsigned vl = (lane >> 5) * 256 + (lane & 15) * 16;
    u32x4 v0 = v_init(lane), v1 = v0;
    o = f32x16{}; m = NEG_BIG;
    for (int i = 0; i < n; ++i) {
        const int t = t0 + i * step;
        u32x4 kr = ld16_dev(Kg + (size_t)t * 1024, lane * 16), a0 = ld16_dev(Vg + (size_t)t * 1024, vl), a1 = ld16_dev(Vg + (size_t)t * 1024, vl + 512);
        asm volatile("s_waitcnt vmcnt(0)" : "+v"(kr), "+v"(a0), "+v"(a1) :: "memory");
        const bf16x8 kf = __builtin_bit_cast(bf16x8, kr);
        if ((lane & 16) == 0) { v0 = a0; v1 = a1; }
        f32x16 s = mfma(kf, q, f32x16{});
        mask_tile(s, i == 0 ? first_mode : (i == n - 1 ? last_mode : 0), z);
        const float mn = fmaxf(m, xhalf_max(max16(s)));
        const float al = __builtin_amdgcn_exp2f(m - mn);
        m = mn;
#pragma unroll
        for (int r = 0; r < 16; ++r) { o[r] *= al; s[r] = __builtin_amdgcn_exp2f(s[r] - mn); }
        pv_regs(s, o, v0, v1);
    }
}

#define T_EXP(C, i) "v_exp_f32 v[" #C "+" #i "], v[" #C "+" #i "]\n\t"
#define T_CVT(C, d, a, b) "v_cvt_pk_bf16_f32 v[" #C "+" #d "], v[" #C "+" #a "], v[" #C "+" #b "]\n\t"
#define T_WAIT(n) "s_waitcnt lgkmcnt(" #n ")\n\t"
#define T_QK(N, CR) "v_mfma_f32_32x32x16_bf16 v[" #N ":" #N "+15], v[68:71], v[64:67], v[" #CR ":" #CR "+15]\n\t"
#define T_KRD(KOFF) "ds_read_b128 v[68:71], v30 offset:" #KOFF "\n\t"
#define X_TILE(C, PV1P, QKN, W0, VRD) \
    T_EXP(C, 0) T_EXP(C, 1) T_EXP(C, 2) T_EXP(C, 3) \
    PV1P \
    T_EXP(C, 4) T_EXP(C, 5) T_EXP(C, 6) T_EXP(C, 7) T_EXP(C, 8) \
    QKN \
    T_CVT(C, 0, 0, 1) T_CVT(C, 1, 2, 3) T_CVT(C, 2, 4, 5) T_CVT(C, 3, 6, 7) T_EXP(C, 9) T_EXP(C, 10) \
    W0 \
    "v_mfma_f32_32x32x16_bf16 v[96:111], v[72:75], v[" #C ":" #C "+3], v[96:111]\n\t" \
    VRD \
    T_EXP(C, 11) T_EXP(C, 12) T_EXP(C, 13) T_EXP(C, 14) T_EXP(C, 15) T_CVT(C, 4, 8, 9) T_CVT(C, 5, 10, 11) T_CVT(C, 6, 12, 13) T_CVT(C, 7, 14, 15)
#define T_PV1(P) "v_mfma_f32_32x32x16_bf16 v[96:111], v[76:79], v[" #P "+4:" #P "+7], v[96:111]\n\t"
#define T_XON "s_mov_b64 exec, %[msk]\n\t"
#define T_XOFF "s_mov_b64 exec, -1\n\t"
#define T_V0(O) "ds_read_b128 v[72:75], v31 offset:" #O "\n\t"
#define T_V1(O) "ds_read_b128 v[76:79], v31 offset:" #O "+512\n\t"
#define A_TILE(C, N, KOFF, V1OFF, V0OFF) X_TILE(C, T_WAIT(1) T_PV1(N), T_QK(N, 80) T_KRD(KOFF), T_WAIT(1), T_XON T_V1(V1OFF) T_V0(V0OFF) T_XOFF)
#define A_TILE_FIRST(C, N, KOFF, V1OFF, V0OFF) X_TILE(C, "", T_WAIT(1) T_QK(N, 80) T_KRD(KOFF), T_WAIT(1), T_XON T_V1(V1OFF) T_V0(V0OFF) T_XOFF)
#define T_DRAIN(P, X) T_WAIT(0) "s_nop 1\n\t" T_PV1(P) X "s_nop 15\n\t"

DI void run_b9(unsigned kaddr, unsigned vaddr, bf16x8 q, f32x16 s0, const f32x16& nm, const f32x16& cfar, f32x16& o, u32x4 v0) {
    u32x4 v1 = v0, kf;
    f32x16 s1;
    asm volatile(
        T_KRD(7168) T_XON T_V0(8192) T_XOFF
        A_TILE_FIRST(32, 48, 6144, 8192, 7168)
        A_TILE(48, 32, 5120, 7168, 6144)
        A_TILE(32, 48, 4096, 6144, 5120)
        A_TILE(48, 32, 3072, 5120, 4096)
        A_TILE(32, 48, 2048, 4096, 3072)
        A_TILE(48, 32, 1024, 3072, 2048)
        A_TILE(32, 48, 0, 2048, 1024)
        X_TILE(48, T_WAIT(1) T_PV1(32), T_QK(32, 112), T_WAIT(0), T_XON T_V1(1024) T_V0(0) T_XOFF)
        X_TILE(32, T_WAIT(1) T_PV1(48), "", T_WAIT(0), T_XON T_V1(0) T_XOFF)
        T_DRAIN(32, "")
        : "+{v[32:47]}"(s0), "=&{v[48:63]}"(s1), "=&{v[68:71]}"(kf), "+{v[72:75]}"(v0), "+{v[76:79]}"(v1), "+{v[96:111]}"(o)
        : "{v30}"(kaddr), "{v31}"(vaddr), "{v[64:67]}"(q), "{v[80:95]}"(nm), "{v[112:127]}"(cfar), [msk] "s"(0x0000FFFF0000FFFFull)
        : "memory");
}

#define A_PAIR(K0) A_TILE(48, 32, K0+2048, K0, K0+1024) A_TILE(32, 48, K0+3072, K0+1024, K0+2048)
#define A_RUN_HEAD \
        "s_setprio 1\n\t" \
        "ds_read_b128 v[68:71], v30\n\t" \
        "s_waitcnt lgkmcnt(0)\n\t" \
        "v_mfma_f32_32x32x16_bf16 v[32:47], v[68:71], v[64:67], v[80:95]\n\t" \
        T_KRD(1024) T_XON T_V0(0) T_XOFF \
        "s_nop 7\n\t" \
        "s_nop 3\n\t" \
        A_TILE_FIRST(32, 48, 2048, 0, 1024)
#define A_RUN_TAIL(LAST) X_TILE(48, T_WAIT(1) T_PV1(32), "", T_WAIT(0), T_XON T_V1(LAST) T_XOFF) T_DRAIN(48, "")
#define A_RUN_OPS \
        : "=&{v[32:47]}"(s0), "=&{v[48:63]}"(s1), "=&{v[68:71]}"(kf), "+{v[72:75]}"(v0), "+{v[76:79]}"(v1), "+{v[96:111]}"(o) \
        : "{v30}"(kaddr), "{v31}"(vaddr), "{v[64:67]}"(q), "{v[80:95]}"(nm), [msk] "s"(0x0000FFFF0000FFFFull) \
        : "memory"
template <int NT>
DI void run_a(unsigned kaddr, unsigned vaddr, bf16x8 q, const f32x16& nm, f32x16& o, u32x4 v0) {
    u32x4 v1 = v0;
    f32x16 s0, s1;
    u32x4 kf;
    if constexpr (NT == 8)
        asm volatile(A_RUN_HEAD A_PAIR(1024) A_PAIR(3072) A_PAIR(5120) A_RUN_TAIL(7168) A_RUN_OPS);
    else
        asm volatile(A_RUN_HEAD A_PAIR(1024) A_PAIR(3072) A_PAIR(5120) A_PAIR(7168) A_PAIR(9216) A_PAIR(11264) A_PAIR(13312) A_PAIR(15360)
                     A_PAIR(17408) A_PAIR(19456) A_PAIR(21504) A_RUN_TAIL(23552) A_RUN_OPS);
}

DI void store_o(char* ws, int br, int qtile, int head, int lane, const f32x16& o) {
    const float inv = __builtin_amdgcn_rcpf(o[8]);
    u32x4 v;
#pragma unroll
    for (int i = 0; i < 4; ++i) v[i] = cvtpk(o[2 * i] * inv, o[2 * i + 1] * inv);
    st16_wt(ws + WS_O + (size_t)br * TB + (size_t)(qtile * 32 + head) * 1024 + lane * 16, v);
}
DI void put_partial(LDSP float* ex, const f32x16& o, float m) {
#pragma unroll
    for (int r = 0; r < 9; ++r) ex[r * 64] = o[r];
    ex[9 * 64] = m;
}
DI void merge_partial(const LDSP float* ex, f32x16& o, float m) {
    const float m1 = ex[9 * 64], mn = fmaxf(m, m1);
    const float s0 = __builtin_amdgcn_exp2f(m - mn), s1 = __builtin_amdgcn_exp2f(m1 - mn);
#pragma unroll
    for (int r = 0; r < 9; ++r) o[r] = o[r] * s0 + ex[r * 64] * s1;
}
DI unsigned l_ex(int j) { return j < 6 ? 98304 + j * 2560 : 148480 + (j - 6) * 2560; }
DI unsigned l_wob(int q) { return q < 2 ? 114688 + q * 16384 : (q - 2) * 16384; }
DI unsigned l_woa(int q) { return 32768 + q * 16384; }
DI unsigned l_red(int br, int g, int oq) { return l_wob(g) + oq * 8192; }
DI void wo_issue(const char* Wo, int nb, int p, unsigned dst, int lane, unsigned lds0) {
    const int r = lane >> 3, sl = (lane & 7) ^ (((r >> 1) & 1) << 2);
    glds16(Wo + (size_t)p * 8192, (unsigned)r * 1024u + nb * 128 + sl * 16, lds0 + dst);
}

template <int SLEEP = 1>
DI void group_sync(LDSP char* ctr, unsigned target, int lane) {
    asm volatile("s_waitcnt lgkmcnt(0)" ::: "memory");
    if (lane == 0) __hip_atomic_fetch_add((LDSP unsigned*)ctr, 1u, __ATOMIC_RELAXED, __HIP_MEMORY_SCOPE_WORKGROUP);
    if ((unsigned)rfl(*(volatile LDSP unsigned*)ctr) < target) {
#pragma unroll 1
        for (int it = 0; it < SPIN_MAX; ++it) {
            __builtin_amdgcn_s_sleep(SLEEP);
            if ((unsigned)rfl(*(volatile LDSP unsigned*)ctr) >= target) break;
        }
    }
    asm volatile("" ::: "memory");
}

template <int SLEEP>
DI void lds_await_count(LDSP char* ctr, unsigned target) {
#pragma unroll 1
    for (int it = 0; it < SPIN_MAX; ++it) {
        if ((unsigned)rfl(*(volatile LDSP unsigned*)ctr) >= target) break;
        __builtin_amdgcn_s_sleep(SLEEP);
    }
    asm volatile("" ::: "memory");
}
template <bool FIRST>
DI void out_tile(u32x4 (&of)[8], unsigned wlo, unsigned whi, char* out, int br, int qb, int nb, int w, LDSP char* lds, LDSP char* gate, unsigned gate_n, LDSP char* ctr, unsigned gs) {
    const int lane = fresh_lane(), oq = w & 1, kq = (w >> 1) & 3;
    const int h = lane >> 5, g1 = (lane >> 4) & 1, q = (lane & 15) >> 2, p = lane & 3;
    unsigned wa[2][2];
#pragma unroll
    for (int j = 0; j < 2; ++j) {
        const int nt = j ^ (kq >> 1);
        const unsigned l = (unsigned)(4 * h + q) * 128u + (((nt * 32 + 16 * (p & 1) + 8 * (g1 ^ (kq & 1)) + 4 * (p >> 1)) * 2) ^ (((q >> 1) & 1) << 6));
        wa[0][j] = wlo + l; wa[1][j] = whi + l;
    }
    f32x16 acc[2] = {f32x16{}, f32x16{}};
    bf16x8 wf[2][2];
#pragma unroll
    for (int j = 0; j < 2; ++j) wf[0][j] = cat44(lds_tr(lds + wa[0][j]), lds_tr(lds + wa[0][j] + 1024));
#pragma unroll
    for (int i = 0; i < 8; ++i) {
        if (i < 7) {
#pragma unroll
            for (int j = 0; j < 2; ++j) wf[(i + 1) & 1][j] = cat44(lds_tr(lds + wa[(i + 1) >> 2][j] + (i + 1) * 2048), lds_tr(lds + wa[(i + 1) >> 2][j] + (i + 1) * 2048 + 1024));
        }
        asm volatile("s_waitcnt vmcnt(%1)" : "+v"(of[i]) : "i"(7 - i) : "memory");
#pragma unroll
        for (int j = 0; j < 2; ++j) acc[j] = mfma(wf[i & 1][j], __builtin_bit_cast(bf16x8, of[i]), acc[j]);
    }
    if (FIRST) {
        asm volatile("s_waitcnt lgkmcnt(0)" ::: "memory");
        if (lane == 0) __hip_atomic_fetch_add((LDSP unsigned*)gate, 1u, __ATOMIC_RELAXED, __HIP_MEMORY_SCOPE_WORKGROUP);
    }
    lds_await_count<1>(gate, gate_n);
#pragma unroll
    for (int x = 1; x < 4; ++x) {
        LDSP f32x4* e = (LDSP f32x4*)(lds + l_red(br, kq ^ x, oq) + (x - 1) * 2048) + lane;
        const int j = x >> 1, u = x & 1;
        e[0] = f32x4{acc[j][8 * u], acc[j][8 * u + 1], acc[j][8 * u + 2], acc[j][8 * u + 3]};
        e[64] = f32x4{acc[j][8 * u + 4], acc[j][8 * u + 5], acc[j][8 * u + 6], acc[j][8 * u + 7]};
    }
    group_sync<1>(ctr, gs + 8, lane);
    float r[8];
#pragma unroll
    for (int x = 0; x < 8; ++x) r[x] = acc[0][x];
#pragma unroll
    for (int k = 0; k < 3; ++k) {
        const LDSP f32x4* e = (const LDSP f32x4*)(lds + l_red(br, kq, oq) + k * 2048) + lane;
        const f32x4 a = e[0], b = e[64];
#pragma unroll
        for (int x = 0; x < 4; ++x) { r[x] += a[x]; r[4 + x] += b[x]; }
    }
    if (FIRST) {
        asm volatile("s_waitcnt lgkmcnt(0)" : "+v"(r[0]), "+v"(r[1]), "+v"(r[2]), "+v"(r[3]), "+v"(r[4]), "+v"(r[5]), "+v"(r[6]), "+v"(r[7]) :: "memory");
        if (lane == 0) __hip_atomic_fetch_add((LDSP unsigned*)gate, 1u, __ATOMIC_RELAXED, __HIP_MEMORY_SCOPE_WORKGROUP);
    }
    u32x4 v;
#pragma unroll
    for (int i = 0; i < 4; ++i) v[i] = cvtpk(r[2 * i], r[2 * i + 1]);
    *(u32x4*)(out + (size_t)((qb * 2 + oq) * 32 + (lane & 31)) * 2048 + br * 1024 + nb * 128 + (kq >> 1) * 64 + h * 32 + (kq & 1) * 16) = v;
}
DI void load_o(u32x4 (&of)[8], const char* ws, int br, int qb, int w, int lane) {
    const char* Of = ws + WS_O + (size_t)br * TB + (size_t)(qb * 2 + (w & 1)) * 32768 + ((w >> 1) & 3) * 8192;
#pragma unroll
    for (int i = 0; i < 8; ++i) of[i] = ld16_dev(Of + i * 1024, lane * 16);
}

DI void phase_attn(char* ws, const char* WoA, const char* WoB, char* out, int qb, int nb, LDSP char* lds, unsigned token) {
    const unsigned lds0 = (unsigned)(uintptr_t)lds;
    int lane = fresh_lane();
    const int w = rfl(threadIdx.x >> 6);
    const int bid = blockIdx.x, head = (bid & 7) * 4 + (bid >> 6), jb = (bid >> 3) & 7;
    const int qt = w & 7, half = w >> 3, Qt = jb * 8 + qt;
    const char* KA = ws + WS_K + (size_t)head * 65536;
    const char* VA = ws + WS_V + (size_t)head * 65536;
    const char* qp = ws + WS_Q + (size_t)(head * 64 + Qt) * 1024;
    if (half == 1) {
        __builtin_amdgcn_s_setprio(3);
        lds_await<1>(lds + L_C + 8, ~token);
#pragma unroll
        for (int i = 0; i < 6; ++i) {
            const int h = i & 1, t = (i >> 1) * 8 + qt;
            glds16_dev(KA + (size_t)(h * 32 + 8 + t) * 1024, lane * 16, lds0 + h * 49152 + t * 1024);
            glds16_dev(VA + (size_t)(h * 32 + 8 + t) * 1024, lane * 16, lds0 + h * 49152 + 24576 + t * 1024);
        }
    }
    lds_await_count<1>(lds + L_C + 12, 4);
    __builtin_amdgcn_s_setprio(1);
    const bf16x8 qa = lds_frag(lds + L_QA + qt * 1024 + lane * 16);
    f32x16 oa = f32x16{};
    float ma = xhalf_max(max16(mfma(lds_frag(lds + L_LEAD + half * 16384 + lane * 16), qa, f32x16{})));
    const f32x16 nma = bcast16(-ma, lane);
    run_a<8>(lds0 + L_LEAD + half * 16384 + lane * 16, lds0 + L_LEAD + half * 16384 + 8192 + (lane >> 5) * 256 + (lane & 15) * 16, qa, nma, oa, v_init(lane));
    asm volatile("s_waitcnt vmcnt(0)" ::: "memory");
    if (w >= 4 && w < 8) {
        const int xcd = bid & 7, idx = bid >> 3, tb = (xcd & 3) * 4 + (idx & 3), cb = (xcd >> 2) * 8 + (idx >> 2);
        publish(lds + L_C + 4, 3, ws + F_G + (128 + cb) * 128 + tb * 4, 8, 2048, token, lane);
    }
    asm volatile("s_barrier" ::: "memory");
    lane = fresh_lane();
    if (w == 15) {
        __builtin_amdgcn_s_setprio(3);
        poll_flags(b_flag(ws, head, jb, lane), token);
        stage_window<16>(ws, lds0, head, jb, 0, lane);
        asm volatile("s_waitcnt vmcnt(0)" ::: "memory");
        lds_signal(lds + L_T + 32, 1);
        lane = fresh_lane();
    }
    run_a<24>(lds0 + half * 49152 + lane * 16, lds0 + half * 49152 + 24576 + (lane >> 5) * 256 + (lane & 15) * 16, qa, nma, oa, v_init(lane));
    if (half) __builtin_amdgcn_s_setprio(3); else __builtin_amdgcn_s_setprio(2);
    lane = fresh_lane();
    LDSP float* ex = (LDSP float*)(lds + l_ex(qt)) + lane;
    if (__any(unsafe_state(oa))) safe_tiles(KA, VA, half * 32, 32, 1, 0, 0, qa, lane, oa, ma);
    const int kq = (w >> 1) & 3;
    u32x4 of[8];
    if (half == 1) {
        if (lane == 0) __hip_atomic_fetch_add((LDSP unsigned*)(lds + L_T + 56), 1u, __ATOMIC_RELAXED, __HIP_MEMORY_SCOPE_WORKGROUP);
        lds_await<1>(lds + L_T + 4 * qt, 0);
        merge_partial(ex, oa, ma);
        store_o(ws, 0, Qt, head, lane, oa);
        publish(lds + L_T + 44, 7, ws + F_O + jb * 128 + head * 4, 4, 1024, token, lane);
        lds_await<1>(lds + L_T + 48, ~token);
        load_o(of, ws, 1, qb, w, lane);
        lds_await_count<1>(lds + L_T + 60, 8);
        out_tile<true>(of, l_wob(kq), l_wob(kq), out, 1, qb, nb, w, lds, lds + L_T + 68, 8, lds + L_T + 64, 0);
        return;
    }
    put_partial(ex, oa, ma);
    lds_signal(lds + L_T + 4 * qt, 1);
    {
        lds_await<2>(lds + L_T + 32, 0);
        u32x4 qbr = ld16_dev(qp + TB, lane * 16);
        asm volatile("s_waitcnt vmcnt(0)" : "+v"(qbr) :: "memory");
        const bf16x8 qbf = __builtin_bit_cast(bf16x8, qbr);
        const int lo = lane * 16, z = (lane & 31) - 4 * (lane >> 5);
        const unsigned vl = (lane >> 5) * 256 + (lane & 15) * 16;
        const int t0 = qt + 8, nt = jb ? 9 : qt + 1;
        f32x16 o = f32x16{};
        f32x16 s = mfma(lds_frag(lds + L_BK + t0 * 1024 + lo), qbf, f32x16{});
        mask_tile(s, 1, z);
        float m = max16(s);
        if (nt > 1) m = fmaxf(m, max16(mfma(lds_frag(lds + L_BK + (t0 - 1) * 1024 + lo), qbf, f32x16{})));
        m = xhalf_max(m);
        const f32x16 nm = bcast16(-m, lane);
        if (jb) {
#pragma unroll
            for (int r = 0; r < 16; ++r) s[r] -= m;
            f32x16 cfar = nm;
            mask_tile(cfar, 2, z);
            run_b9(lds0 + L_BK + qt * 1024 + lo, lds0 + L_BV + qt * 1024 + vl, qbf, s, nm, cfar, o, v_init(lane));
        } else {
            u32x4 v0 = v_init(lane), v1 = v0;
            for (int i = 0; i < nt; ++i) {
                f32x16 p = mfma(lds_frag(lds + L_BK + (t0 - i) * 1024 + lo), qbf, nm);
                if (i == 0) mask_tile(p, 1, z);
#pragma unroll
                for (int r = 0; r < 16; ++r) p[r] = __builtin_amdgcn_exp2f(p[r]);
                load_v(v0, v1, lds0 + L_BV + (t0 - i) * 1024 + vl);
                pv_regs(p, o, v0, v1);
            }
        }
        lane = fresh_lane();
        if (__any(unsafe_state(o)))
            safe_tiles(ws + WS_K + TB + (size_t)head * 65536, ws + WS_V + TB + (size_t)head * 65536, Qt, nt, -1, 1, jb ? 2 : 0, qbf, lane, o, m);
        store_o(ws, 1, Qt, head, lane, o);
        publish(lds + L_T + 40, 7, ws + F_O + (32 + jb) * 128 + head * 4, 4, 1024, token, lane);
    }
    group_sync<8>(lds + L_T + 36, 8, lane);
#pragma unroll
    for (int i = 0; i < 8; ++i) { const int p = w * 8 + i; wo_issue(WoB, nb, p, l_wob(p >> 4) + (p & 15) * 1024, lane, lds0); }
#pragma unroll
    for (int i = 0; i < 2; ++i) { const int p = w * 2 + i; wo_issue(WoA, nb, p, l_woa(0) + p * 1024, lane, lds0); }
    asm volatile("s_waitcnt vmcnt(2)" ::: "memory");
    if (lane == 0) __hip_atomic_fetch_add((LDSP unsigned*)(lds + L_T + 60), 1u, __ATOMIC_RELAXED, __HIP_MEMORY_SCOPE_WORKGROUP);
    if (w == 0) {
        asm volatile("s_waitcnt vmcnt(0)" ::: "memory");
        poll_flags(ws + F_O + ((4 + (nb & 3)) * 8 + (qb >> 2)) * 128 + (lane & 31) * 4, token);
        lds_signal(lds + L_T + 48, token);
    }
    lds_await_count<6>(lds + L_T + 56, 8);
    if (w) {
#pragma unroll
        for (int i = 0; i < 7; ++i) { const int p = 16 + i * 7 + w - 1; if (p < 64) wo_issue(WoA, nb, p, l_woa(0) + p * 1024, lane, lds0); }
    }
    asm volatile("s_waitcnt vmcnt(0)" ::: "memory");
    group_sync<2>(lds + L_T + 36, 16, lane);
    lds_await_count<2>(lds + L_T + 44, 8);
    if (w == 0) {
        poll_flags(ws + F_O + ((nb & 3) * 8 + (qb >> 2)) * 128 + (lane & 31) * 4, token);
        lds_signal(lds + L_T + 52, token);
    } else lds_await<1>(lds + L_T + 52, ~token);
    load_o(of, ws, 0, qb, w, lane);
    out_tile<false>(of, l_woa(kq), l_woa(kq), out, 0, qb, nb, w, lds, lds + L_T + 68, 16, lds + L_T + 36, 16);
}

__global__ __launch_bounds__(1024) void k_main(const char* xA, const char* wA, const char* oA, const char* xB, const char* wB, const char* oB, char* ws, char* out) {
    extern __shared__ __attribute__((aligned(16))) char smem[];
    LDSP char* lds = (LDSP char*)smem;
    const int bid = blockIdx.x, xcd = bid & 7, idx = bid >> 3;
    const unsigned token = phase_qkv(xA, wA, xB, wB, ws, lds);
    phase_attn(ws, oA, oB, out, xcd * 4 + (idx & 3), idx >> 2, lds, token);
    if (bid == 0 && threadIdx.x < 64) {
        const int lane = threadIdx.x;
#pragma unroll
        for (int i = 0; i < 4; ++i) poll_flags(ws + F_G + (i * 4 + (lane >> 4)) * 128 + (lane & 15) * 4, token);
        if (lane == 0) st_flag(ws + F_EPOCH, token);
    }
}

extern "C" void kernel_launch(void* const* d_in, const int* in_sizes, int n_in, void* d_out, int out_size, void* d_ws, size_t ws_size,
                              hipStream_t stream) {
    const char* xA = (const char*)d_in[0];
    const char* wA = (const char*)d_in[1];
    const char* oA = (const char*)d_in[2];
    const char* xB = (const char*)d_in[3];
    const char* wB = (const char*)d_in[4];
    const char* oB = (const char*)d_in[5];
    char* ws = (char*)d_ws;
    char* out = (char*)d_out;
    (void)hipFuncSetAttribute((const void*)k_main, hipFuncAttributeMaxDynamicSharedMemorySize, 163840);
    k_main<<<256, 1024, 163840, stream>>>(xA, wA, oA, xB, wB, oB, ws, out);
}
```
